```python
import torch

def _softmax(x, axis=-1):
    return torch.softmax(x, dim=axis)
def _gelu(x):
    return torch.nn.functional.gelu(x, approximate="tanh")
def _silu(x):
    return torch.nn.functional.silu(x)
def _sigmoid(x):
    return torch.sigmoid(x)
def _layernorm(x, axis=-1, eps=1e-5):
    m = x.mean(dim=axis, keepdim=True)
    v = x.var(dim=axis, keepdim=True, unbiased=False)
    return (x - m) / torch.sqrt(v + eps)
def _scatter_add(tgt, idx, src, axis=0):
    return tgt.clone().index_add_(axis, idx, src)
def _segment_sum(src, seg, n):
    out = torch.zeros((n,) + src.shape[1:], dtype=src.dtype)
    return out.index_add_(0, seg, src)
def _pad(x, pw):
    flat = tuple(v for pair in reversed(pw) for v in pair)
    return torch.nn.functional.pad(x, flat)

kernel_name = 'sequential_preLN__gqa_block_cached__dense_activation_silu__decode'

def setup_inputs():

    _w = (lambda a: (lambda t: t.to(torch.bfloat16) if t.is_floating_point() else t)(torch.as_tensor(a)))
    def _A_setup():
        B, Sq, D = 2, 1, 448
        Hq, Hkv, Sk, Dh = 16, 8, 4096, 256
        assert Hq % Hkv == 0, (Hq, Hkv)
        return {
            "x": _w((lambda *s: torch.randn(*s, dtype=torch.bfloat16) * 0.1)(B, Sq, D)),
            "k_cache": _w((lambda *s: torch.randn(*s, dtype=torch.bfloat16) * 1.6)(B, Hkv, Sk, Dh)),
            "v_cache": _w((lambda *s: torch.randn(*s, dtype=torch.bfloat16) * 1.6)(B, Hkv, Sk, Dh)),
            "wq": _w((lambda *s: torch.randn(*s, dtype=torch.bfloat16) * 1.6)(D, Hq * Dh) * (D ** -0.5)),
            "wo": _w((lambda *s: torch.randn(*s, dtype=torch.bfloat16) * 1.6)(Hq * Dh, D) * ((Hq * Dh) ** -0.5)),
        }
    _A_in = _A_setup()

    def _B_setup():
        B, S, D, F = 2, 1, 448, 1792
        return {
            "x": _w((lambda *s: torch.randn(*s, dtype=torch.bfloat16) * 0.1)(B, S, D)),
            "w1": _w((lambda *s: torch.randn(*s, dtype=torch.bfloat16) * 3.2)(D, F) * (D ** -0.5)),
            "w2": _w((lambda *s: torch.randn(*s, dtype=torch.bfloat16) * 3.2)(F, D) * (F ** -0.5)),
        }
    _B_in = _B_setup()

    return {
        "A_x": _A_in["x"],
        "A_k_cache": _A_in["k_cache"],
        "A_v_cache": _A_in["v_cache"],
        "A_wq": _A_in["wq"],
        "A_wo": _A_in["wo"],
        "B_x": _B_in["x"],
        "B_w1": _B_in["w1"],
        "B_w2": _B_in["w2"],
    }

def reference(A_x, A_k_cache, A_v_cache, A_wq, A_wo, B_x, B_w1, B_w2):
    def _A_ref(x, k_cache, v_cache, wq, wo):
        B, Sq, D = x.shape
        Hq, Dh = 16, 256
        Hkv = k_cache.shape[1]
        rep = Hq // Hkv
        q = (lambda x, *d: x.permute(*d))((x @ wq).reshape(B, Sq, Hq, Dh), 0, 2, 1, 3)
        k = (lambda x, n, axis: torch.repeat_interleave(x, n, dim=axis))(k_cache, rep, axis=1)
        v = (lambda x, n, axis: torch.repeat_interleave(x, n, dim=axis))(v_cache, rep, axis=1)
        scores = torch.einsum("bhqd,bhkd->bhqk", q, k) * (Dh ** -0.5)
        p = _softmax(scores, axis=-1)
        o = torch.einsum("bhqk,bhkd->bhqd", p, v)
        return (lambda x, *d: x.permute(*d))(o, 0, 2, 1, 3).reshape(B, Sq, Hq * Dh) @ wo

    def _B_ref(x, w1, w2):
        h = torch.einsum("bsd,df->bsf", x, w1)
        h = _silu(h)
        out = torch.einsum("bsf,fd->bsd", h, w2)
        return out

    x = A_x
    h = x + _A_ref(_layernorm(x, axis=-1), A_k_cache, A_v_cache, A_wq, A_wo)
    h2 = h + _B_ref(_layernorm(h, axis=-1), B_w1, B_w2)
    return h2
```

```cpp
#define WIN 6
#define PRETOUCH 0
#include <hip/hip_runtime.h>
#include <stdint.h>

typedef unsigned short u16;
typedef unsigned u32;
typedef unsigned long long u64;
typedef u32 u32x4 __attribute__((ext_vector_type(4)));
typedef u32 u32x2 __attribute__((ext_vector_type(2)));
typedef float f32x4 __attribute__((ext_vector_type(4)));
typedef float f32x2 __attribute__((ext_vector_type(2)));
typedef __bf16 bf16x8 __attribute__((ext_vector_type(8)));
typedef __bf16 bf16x2 __attribute__((ext_vector_type(2)));
#define GAS __attribute__((address_space(1)))
#define LAS __attribute__((address_space(3)))
typedef GAS u64 gu64;
#define RLX_AGENT __ATOMIC_RELAXED, __HIP_MEMORY_SCOPE_AGENT
#define RLX_WG __ATOMIC_RELAXED, __HIP_MEMORY_SCOPE_WORKGROUP

#define WDEFER 2
#define WODEFER 1
#define QTRIG 6
#define OPENW 1
#define XCDMAP 0
#define SLOWS 4
#define NOSTREAM 0
#define G_BASE 0
static_assert(QTRIG + WIN - 2 < 16, "trigger inside the K loop");

constexpr int NT = 1024;
constexpr int SPIN_CAP = 1 << 16;
constexpr int G_EPOCH = 0;
constexpr int G_QP = 512;
constexpr int G_P1 = G_QP + 32768;
constexpr int G_S1 = G_P1 + 131072;
constexpr int G_H = G_S1 + 1024;
constexpr int G_END = G_H + 512 * 57;
constexpr int G_STAMP = 1 << 20;
constexpr int L_K = 0;
constexpr int L_RED = 65536;
constexpr int L_WQ = 131072;
constexpr int L_XN = L_WQ + 14336;
constexpr int L_Q = L_XN + 512;
constexpr int L_P = L_Q + 1024;
constexpr int L_SM = L_P + 1024;
constexpr int L_CNT = L_SM + 128;
constexpr int L_TAG = L_CNT + 64;
constexpr int L_OB = L_TAG + 16;
constexpr int L_UR = L_OB + 128;
constexpr int L_FB = L_UR + 8 * 320;
constexpr int L_END = L_FB + 1024;
enum { C_Q = 0, C_K, C_WQ, C_KT, C_MAX, C_P, C_VISS, C_DONE, C_W, C_N };

struct Params {
  const u16* x; const u16* kc; const u16* vc; const u16* wq; const u16* wo; const u16* w1; const u16* w2; u16* out; u64* ws;
};

__device__ __forceinline__ float asf(u32 u) { return __builtin_bit_cast(float, u); }
__device__ __forceinline__ u32 asu(float f) { return __builtin_bit_cast(u32, f); }
__device__ __forceinline__ float bflo(u32 u) { return asf(u << 16); }
__device__ __forceinline__ float bfhi(u32 u) { return asf(u & 0xffff0000u); }
__device__ __forceinline__ u16 f2bf(float f) { return __builtin_bit_cast(u16, (__bf16)f); }
__device__ __forceinline__ float rbf(float f) { return asf((u32)f2bf(f) << 16); }
__device__ __forceinline__ u32 pk2bf(float lo, float hi) { bf16x2 v = {(__bf16)lo, (__bf16)hi}; return __builtin_bit_cast(u32, v); }
template <int CTRL> __device__ __forceinline__ float dpp(float x) {
  return __builtin_bit_cast(float, __builtin_amdgcn_mov_dpp(__builtin_bit_cast(int, x), CTRL, 0xf, 0xf, true));
}
__device__ __forceinline__ float x16_sum(float x) { auto s = __builtin_amdgcn_permlane16_swap(asu(x), asu(x), false, false); return asf(s[0]) + asf(s[1]); }
__device__ __forceinline__ float x32_sum(float x) { auto s = __builtin_amdgcn_permlane32_swap(asu(x), asu(x), false, false); return asf(s[0]) + asf(s[1]); }
__device__ __forceinline__ float x16_max(float x) { auto s = __builtin_amdgcn_permlane16_swap(asu(x), asu(x), false, false); return fmaxf(asf(s[0]), asf(s[1])); }
__device__ __forceinline__ float row_sum(float x) { x += dpp<0xB1>(x); x += dpp<0x4E>(x); x += dpp<0x141>(x); x += dpp<0x128>(x); return x; }
__device__ __forceinline__ float row_max(float x) {
  x = fmaxf(x, dpp<0xB1>(x)); x = fmaxf(x, dpp<0x4E>(x)); x = fmaxf(x, dpp<0x141>(x)); x = fmaxf(x, dpp<0x128>(x)); return x;
}
__device__ __forceinline__ float wave_sum(float x) { return x32_sum(x16_sum(row_sum(x))); }
__device__ __forceinline__ f32x4 mfma16(u32x4 a, u32x4 b, f32x4 c) {
  return __builtin_amdgcn_mfma_f32_16x16x32_bf16(__builtin_bit_cast(bf16x8, a), __builtin_bit_cast(bf16x8, b), c, 0, 0, 0);
}
template <int N> __device__ __forceinline__ void waitvm() { asm volatile("s_waitcnt vmcnt(%0)" ::"i"(N) : "memory"); }
__device__ __forceinline__ void waitlgkm() { asm volatile("s_waitcnt lgkmcnt(0)" ::: "memory"); }
__device__ __forceinline__ void wg_barrier() { asm volatile("s_waitcnt lgkmcnt(0)\n\ts_barrier" ::: "memory"); }
#define POLKV " nt"
#define POLV POLKV
#define POLW " nt"
__device__ __forceinline__ void glds16(const void* sbase, u32 voff, u32 lds_dst) {
  u32 keep;
  asm volatile("s_mov_b32 %0, m0\n\ts_mov_b32 m0, %3\n\ts_nop 0\n\tglobal_load_lds_dwordx4 %1, %2" POLW "\n\ts_mov_b32 m0, %0"
               : "=&s"(keep) : "v"(voff), "s"(sbase), "s"(lds_dst) : "memory");
}
__device__ __forceinline__ void glds16k(const void* sbase, u32 voff, u32 lds_dst) {
  u32 keep;
  asm volatile("s_mov_b32 %0, m0\n\ts_mov_b32 m0, %3\n\ts_nop 0\n\tglobal_load_lds_dwordx4 %1, %2" POLKV "\n\ts_mov_b32 m0, %0"
               : "=&s"(keep) : "v"(voff), "s"(sbase), "s"(lds_dst) : "memory");
}
__device__ __forceinline__ u32x4 tr_frag(u32 a0, u32 a1) {
  u32x2 r0, r1;
  asm volatile("ds_read_b64_tr_b16 %0, %2\n\tds_read_b64_tr_b16 %1, %3\n\ts_waitcnt lgkmcnt(0)" : "=&v"(r0), "=&v"(r1) : "v"(a0), "v"(a1) : "memory");
  return u32x4{r0.x, r0.y, r1.x, r1.y};
}

struct Tag { u32 hi, lo; };
__device__ __forceinline__ void put(gu64* p, float v, Tag t) {
  __hip_atomic_store(p, ((u64)t.hi << 32) | (u64)((asu(v) & 0xffffff00u) | t.lo), RLX_AGENT);
}
__device__ __forceinline__ u32 tag_bad(u64 g, Tag t) { return (((u32)(g >> 32)) ^ t.hi) | (((u32)g ^ t.lo) & 0xffu); }
__device__ __forceinline__ float dec(u64 g) { return asf((u32)g & 0xffffff00u); }
template <int N> __device__ __forceinline__ void sweep(const gu64* p, int S, Tag t, float (&v)[N]) {
  for (int spins = 0; spins < SPIN_CAP; spins++) {
    u64 g[N];
    u32 bh = 0, bl = 0;
#pragma unroll
    for (int k = 0; k < N; k++) g[k] = __hip_atomic_load(p + k * S, RLX_AGENT);
#pragma unroll
    for (int k = 0; k < N; k++) { bh |= (u32)(g[k] >> 32) ^ t.hi; bl |= (u32)g[k] ^ t.lo; v[k] = dec(g[k]); }
    if (__all((bh | (bl & 0xffu)) == 0u)) break;
  }
}
__device__ __forceinline__ void slow_start(const gu64* p, Tag t) {
  for (int spins = 0; spins < SPIN_CAP; spins++) {
    if (__any(tag_bad(__hip_atomic_load(p, RLX_AGENT), t) == 0u)) break;
    __builtin_amdgcn_s_sleep(SLOWS);
  }
}

__device__ __forceinline__ void cnt_add(u32 addr) {
  u64 keep;
  asm volatile("s_waitcnt lgkmcnt(0)\n\ts_mov_b64 %0, exec\n\ts_mov_b64 exec, 1\n\tds_add_u32 %1, %2\n\ts_mov_b64 exec, %0"
               : "=&s"(keep) : "v"(addr), "v"(1u) : "memory");
}
__device__ __forceinline__ void cnt_wait(LAS u32* c, u32 n) {
  while (__hip_atomic_load(c, RLX_WG) < n) __builtin_amdgcn_s_sleep(1);
  asm volatile("" ::: "memory");
}
__device__ __forceinline__ void cnt_spin(LAS u32* c, u32 n) {
  while (__hip_atomic_load(c, RLX_WG) < n) {}
  asm volatile("" ::: "memory");
}

#define STAMP(i) do {} while (0)
#define FLUSH() do {} while (0)

__global__ void __launch_bounds__(NT, 4) fused_block_kernel(Params P) {
  __shared__ __attribute__((aligned(16))) unsigned char smem[L_END];
  LAS unsigned char* L = (LAS unsigned char*)smem;
  const u32 lds0 = (u32)(size_t)smem;
  const int wg = blockIdx.x;
  const int wave = __builtin_amdgcn_readfirstlane(threadIdx.x >> 6), l = threadIdx.x & 63;
  const int n = l & 15, g = l >> 4;
  const int xcd = wg & 7, idx = wg >> 3;
  const int kvh = (wg >> 4) & 7, b = wg >> 7, s = wg & 15;
  const int pair = b * 8 + kvh, aidx = b * 16 + s;
  gu64* ws = (gu64*)P.ws + G_BASE;
  LAS u32* cnt = (LAS u32*)(L + L_CNT);
  const u32 cnt0 = lds0 + L_CNT;

  if (wave < 8) {
    const int w = wave, hf = l >> 5, c = l & 31;
    if (threadIdx.x < 16) cnt[threadIdx.x] = 0u;
    STAMP(0);
    const unsigned char* kb = (const unsigned char*)P.kc + ((size_t)pair * 4096 + s * 256) * 512;
    const unsigned char* vb = (const unsigned char*)P.vc + ((size_t)pair * 4096 + s * 256) * 512;
    const int wr = (w + b) & 7;
    const u32 koff = hf * 512 + ((c ^ ((2 * wr + hf) & 15)) * 16);
    wg_barrier();
#pragma unroll
    for (int i = 0; i < 16; i++) {
      const int gc = ((i + s) & 15) * 8 + wr;
      glds16k(kb + gc * 1024, koff, lds0 + L_K + gc * 1024);
      if (i >= WIN - 1) waitvm<WIN - 1>();
      if (i == QTRIG + WIN - 2) cnt_add(cnt0 + 4 * C_KT);
    }
    u32x4 v[16];
    const u32 voff = g * 512 + n * 16;
    bool open = false;
#pragma unroll
    for (int i = 0; i < 16; i++) {
      const int gc2 = (((i >> 1) + s) & 7) * 8 + wr;
      asm volatile("global_load_dwordx4 %0, %1, %2 offset:%3" POLV : "=v"(v[i]) : "v"(voff), "s"(vb + gc2 * 2048), "i"((i & 1) * 256) : "memory");
      if (i < 15 && !open) waitvm<WIN - 1>();
      if (i == WIN - 2) cnt_add(cnt0 + 4 * C_K);
      if (OPENW && i >= WIN - 2 && i < 15 && !open) {
        const u64 qk = __hip_atomic_load((LAS u64*)(cnt + C_Q), RLX_WG);
        open = __builtin_amdgcn_readfirstlane((u32)qk + (u32)(qk >> 32)) >= 16;
      }
    }
    cnt_add(cnt0 + 4 * C_VISS);
    STAMP(1);
    cnt_wait(cnt + C_P, 8);
    const u32x4 pf = *(LAS u32x4*)(L + L_P + (n >> 3) * 512 + wr * 64 + g * 16);
    const u32x4 zero4 = {0, 0, 0, 0};
    const u32x4 pa0 = (n & 4) ? zero4 : pf, pa1 = (n & 4) ? pf : zero4;
    const Tag tg{*(LAS u32*)(L + L_TAG), *(LAS u32*)(L + L_TAG + 4)};
    gu64* dst = ws + G_P1 + ((kvh * 16 + 2 * w + hf) * 2 + b) * 512 + s * 32 + c;
    asm volatile("" : "+v"(dst));
    u32x4 bf0[8], bf1[8];
#pragma unroll
    for (int t = 0; t < 4; t++) {
      asm volatile("s_waitcnt vmcnt(%4)" : "+v"(v[4 * t]), "+v"(v[4 * t + 1]), "+v"(v[4 * t + 2]), "+v"(v[4 * t + 3]) : "i"(12 - 4 * t) : "memory");
#pragma unroll
      for (int e = 0; e < 8; e++) {
        const u32 sel = (e & 1) ? 0x07060302u : 0x05040100u;
        bf0[e][t] = __builtin_amdgcn_perm(v[4 * t + 2][e >> 1], v[4 * t][e >> 1], sel);
        bf1[e][t] = __builtin_amdgcn_perm(v[4 * t + 3][e >> 1], v[4 * t + 1][e >> 1], sel);
      }
    }
    f32x4 d[8];
#pragma unroll
    for (int e = 0; e < 8; e++) d[e] = mfma16(pa0, bf0[e], f32x4{0.f, 0.f, 0.f, 0.f});
#pragma unroll
    for (int e = 0; e < 8; e++) d[e] = mfma16(pa1, bf1[e], d[e]);
    *(LAS f32x4*)(L + L_RED + w * 2048 + l * 32) = f32x4{d[0][0], d[1][0], d[2][0], d[3][0]};
    *(LAS f32x4*)(L + L_RED + w * 2048 + l * 32 + 16) = f32x4{d[4][0], d[5][0], d[6][0], d[7][0]};
    float o = 0.f;
    for (int spins = 0; spins < (1 << 22); spins++) {
      u32 x[8];
      asm volatile("" ::: "memory");
#pragma unroll
      for (int ww = 0; ww < 8; ww++) x[ww] = *(volatile LAS u32*)(L + L_RED + ww * 2048 + (64 * w + l) * 4);
      const u32 mx = max(max(max(x[0], x[1]), max(x[2], x[3])), max(max(x[4], x[5]), max(x[6], x[7])));
      o = ((asf(x[0]) + asf(x[1])) + (asf(x[2]) + asf(x[3]))) + ((asf(x[4]) + asf(x[5])) + (asf(x[6]) + asf(x[7])));
      if (__all(mx != 0xffffffffu)) break;
    }
    put(dst, o, tg);
    cnt_add(cnt0 + 4 * C_DONE);
    STAMP(3);
    FLUSH();
    return;
  }

  const int h = wave - 8, q4 = n >> 2, p4 = n & 3;
  Tag tg;
  {
    const int cg = 8 * kvh + (aidx >> 2), kq = aidx & 3;
    const unsigned char* wqs = (const unsigned char*)P.wq + (size_t)(112 * kq) * 8192 + 128 * cg;
    if (h < 7) {
#pragma unroll
      for (int d = 0; d < 2; d++) glds16(wqs + (size_t)(16 * h + 8 * d) * 8192, (l >> 3) * 8192 + (l & 7) * 16, lds0 + L_WQ + (2 * h + d) * 1024);
    }
    u32x4 xr = {0, 0, 0, 0};
    if (h == 5 || h == 6) xr = *(const u32x4*)(P.x + (h & 1) * 448 + (l < 56 ? l : 55) * 8);
    if (h == 7) {
      u64 ep;
      u32 junk;
      const gu64* epp = ws + G_EPOCH;
      const unsigned char* tp = (const unsigned char*)P.out;
      const int rr = xcd * 16 + idx, fs = xcd * 8 + (idx - 16);
      if (l == 1) tp = (const unsigned char*)P.vc + ((size_t)pair * 4096 + s * 256) * 512;
      if (l == 2) tp = (const unsigned char*)P.vc + ((size_t)pair * 4096 + s * 256 + 255) * 512;
      if (l == 3) tp = (const unsigned char*)P.kc + ((size_t)pair * 4096 + s * 256 + 255) * 512;
      if (l == 4) tp = (const unsigned char*)(ws + G_P1 + kvh * 16384);
      if (l == 5) tp = (const unsigned char*)(ws + G_P1 + 131071);
      if (l == 6) tp = (const unsigned char*)(ws + G_END - 1);
      if (idx < 16) {
        if (l == 7) tp = (const unsigned char*)P.wo + (size_t)rr * 32 * 896;
        if (l == 8) tp = (const unsigned char*)P.wo + (size_t)rr * 32 * 896 + 28668;
      } else if (idx < 24 && xcd < 7) {
        if (l == 7) tp = (const unsigned char*)P.w1 + fs * 64;
        if (l == 8) tp = (const unsigned char*)P.w1 + (size_t)447 * 3584 + fs * 64;
        if (l == 9) tp = (const unsigned char*)P.w1 + (size_t)224 * 3584 + fs * 64;
        if (l == 10) tp = (const unsigned char*)P.w2 + (size_t)fs * 32 * 896;
        if (l == 11) tp = (const unsigned char*)P.w2 + (size_t)fs * 32 * 896 + 28668;
      }
      asm volatile("global_load_dwordx2 %0, %1, off sc1" : "=v"(ep) : "v"(epp) : "memory");
      wg_barrier();
      asm volatile("s_waitcnt vmcnt(0)" : "+v"(ep) :: "memory");
      const u64 ne = ep * 6364136223846793005ull + 1442695040888963407ull;
      if (l == 0) *(LAS u32x4*)(L + L_TAG) = u32x4{(u32)(ne >> 8), (u32)ne & 0xffu, (u32)ne, (u32)(ne >> 32)};
      cnt_add(cnt0 + 4 * C_WQ);
    } else {
      wg_barrier();
      if (h >= 5) {
        float xv[8];
#pragma unroll
        for (int k = 0; k < 4; k++) { xv[2 * k] = bflo(xr[k]); xv[2 * k + 1] = bfhi(xr[k]); }
        float sm = ((xv[0] + xv[1]) + (xv[2] + xv[3])) + ((xv[4] + xv[5]) + (xv[6] + xv[7]));
        const float mean = wave_sum(l < 56 ? sm : 0.f) * (1.f / 448.f);
        float ss = 0.f;
#pragma unroll
        for (int k = 0; k < 8; k++) { const float d = xv[k] - mean; ss = fmaf(d, d, ss); }
        const float var = rbf(wave_sum(l < 56 ? ss : 0.f) * (1.f / 448.f)), mb = rbf(mean);
        const float rden = __builtin_amdgcn_rcpf(rbf(__builtin_amdgcn_sqrtf(rbf(var + 1e-5f))));
        u32x4 o;
#pragma unroll
        for (int k = 0; k < 4; k++) o[k] = pk2bf(rbf(xv[2 * k] - mb) * rden, rbf(xv[2 * k + 1] - mb) * rden);
        const int rl = l - 14 * kq;
        if (rl >= 0 && rl < 14) *(LAS u32x4*)(L + L_XN + (h & 1) * 256 + rl * 16) = o;
        if (l >= 62) *(LAS u32x4*)(L + L_XN + (h & 1) * 256 + (l - 48) * 16) = u32x4{0, 0, 0, 0};
      }
      waitvm<0>();
      cnt_add(cnt0 + 4 * C_WQ);
    }
    cnt_spin(cnt + C_WQ, 8);
    tg.hi = __builtin_amdgcn_readfirstlane(*(LAS u32*)(L + L_TAG));
    tg.lo = __builtin_amdgcn_readfirstlane(*(LAS u32*)(L + L_TAG + 4));
    if (h < 4) {
      f32x4 acc = {0.f, 0.f, 0.f, 0.f};
#pragma unroll
      for (int ks = 0; ks < 4; ks++) {
        const u32x4 af = *(LAS u32x4*)(L + L_XN + (n & 1) * 256 + ks * 64 + g * 16);
        int r0 = 32 * ks + 8 * g + q4;
        if (ks == 3) r0 = (g >= 2) ? r0 - 16 : r0;
        const u32 a = lds0 + L_WQ + r0 * 128 + 32 * h + 8 * p4;
        acc = mfma16(af, tr_frag(a, a + 4 * 128), acc);
      }
      if (g < 2) put(ws + G_QP + (g * 4 + kq) * 4096 + 64 * cg + 16 * h + n, g ? acc[1] : acc[0], tg);
      STAMP(4);
    }
    cnt_wait(cnt + C_KT, 8);
    float qg[4];
    sweep<4>(ws + G_QP + b * 4 * 4096 + kvh * 512 + 64 * h + l, 4096, tg, qg);
    *(LAS u16*)(L + L_Q + (64 * h + l) * 2) = f2bf((qg[0] + qg[1]) + (qg[2] + qg[3]));
    cnt_add(cnt0 + 4 * C_Q);
    STAMP(5);
  }
  {
    cnt_wait(cnt + C_Q, 8);
    u32x4 qf[8];
#pragma unroll
    for (int j = 0; j < 8; j++) qf[j] = *(LAS u32x4*)(L + L_Q + (n & 1) * 512 + j * 64 + g * 16);
    cnt_wait(cnt + C_K, 8);
    STAMP(6);
    const LAS unsigned char* kr = L + L_K + (32 * h + n) * 512;
    u32x4 kf[16];
#pragma unroll
    for (int j = 0; j < 8; j++) {
      kf[2 * j] = *(const LAS u32x4*)(kr + (((4 * j + g) ^ n) * 16));
      kf[2 * j + 1] = *(const LAS u32x4*)(kr + 8192 + (((4 * j + g) ^ n) * 16));
    }
    __builtin_amdgcn_sched_barrier(0);
    f32x4 acc0 = {0.f, 0.f, 0.f, 0.f}, acc1 = {0.f, 0.f, 0.f, 0.f};
#pragma unroll
    for (int j = 0; j < 8; j++) { acc0 = mfma16(qf[j], kf[2 * j], acc0); acc1 = mfma16(qf[j], kf[2 * j + 1], acc1); }
    const float sr = (g & 1) ? ((g >> 1) ? acc1[1] : acc1[0]) : ((g >> 1) ? acc0[1] : acc0[0]);
    const float sc = rbf(sr) * 0.0625f;
    const float mw = x16_max(row_max(sc));
    LAS float* sm = (LAS float*)(L + L_SM);
    if ((l & 31) == 0) sm[(g >> 1) * 8 + h] = mw;
    cnt_add(cnt0 + 4 * C_MAX);
    cnt_spin(cnt + C_MAX, 8);
    const f32x4 ma = *(LAS f32x4*)(sm + (g >> 1) * 8), mb = *(LAS f32x4*)(sm + (g >> 1) * 8 + 4);
    *(LAS u32x4*)(L + L_RED + h * 2048 + l * 32) = u32x4{~0u, ~0u, ~0u, ~0u};
    *(LAS u32x4*)(L + L_RED + h * 2048 + l * 32 + 16) = u32x4{~0u, ~0u, ~0u, ~0u};
    const float m = fmaxf(fmaxf(fmaxf(ma[0], ma[1]), fmaxf(ma[2], ma[3])), fmaxf(fmaxf(mb[0], mb[1]), fmaxf(mb[2], mb[3])));
    const u16 pb = f2bf(__builtin_amdgcn_exp2f((sc - m) * 1.4426950408889634f));
    const int row = 16 * (g & 1) + n;
    *(LAS u16*)(L + L_P + (g >> 1) * 512 + (row >> 2) * 64 + (row & 3) * 16 + ((h - s) & 7) * 2) = pb;
    const float lw = x16_sum(row_sum(bflo((u32)pb)));
    if ((l & 31) == 0) sm[16 + (g >> 1) * 8 + h] = lw;
    cnt_add(cnt0 + 4 * C_P);
    STAMP(7);
    if (h == 7) {
      cnt_spin(cnt + C_P, 8);
      const f32x4 la = *(LAS f32x4*)(sm + 16 + (g >> 1) * 8), lb = *(LAS f32x4*)(sm + 16 + (g >> 1) * 8 + 4);
      const float ls = ((la[0] + la[1]) + (la[2] + la[3])) + ((lb[0] + lb[1]) + (lb[2] + lb[3]));
      if ((l & 30) == 0) put(ws + G_S1 + (((kvh * 2 + (g >> 1)) * 2 + b) * 16 + s) * 2 + (l & 1), (l & 1) ? ls : m, tg);
    }
  }
  const Tag tgy{tg.hi, tg.lo ^ 1u}, tgo{tg.hi, tg.lo ^ 2u};
  if (idx < 16) {
    const int j = idx, r = xcd * 16 + j;
    gu64* blk = ws + G_P1 + r * 1024;
    if (h < 2) {
      const gu64* pd = blk + h * 512 + l;
      const gu64* ps = ws + G_S1 + ((xcd * 2 + (j >> 3)) * 2 + h) * 32 + (l & 31);
      cnt_wait(cnt + C_DONE, 8);
      float ov[8], wk[8];
      u32 pend = 0x1ffu;
      for (int spins = 0; spins < SPIN_CAP && pend; spins++) {
        u64 gg[9];
#pragma unroll
        for (int k = 0; k < 9; k++) if (pend & (1u << k)) gg[k] = __hip_atomic_load(k < 8 ? pd + k * 64 : ps, RLX_AGENT);
#pragma unroll
        for (int k = 0; k < 8; k++) if (pend & (1u << k)) {
          ov[k] = dec(gg[k]);
          if (__all(tag_bad(gg[k], tg) == 0u)) pend &= ~(1u << k);
        }
        if ((pend & 0x100u) && __all(tag_bad(gg[8], tg) == 0u)) {
          pend &= 0xffu;
          const float st = dec(gg[8]);
          const float M = x16_max(row_max((l & 1) ? -INFINITY : st));
          const float wgt = __builtin_amdgcn_exp2f((st - M) * 1.4426950408889634f);
          const float lw = dpp<0xB1>(wgt) * st;
          const float wn = wgt * __builtin_amdgcn_rcpf(x16_sum(row_sum((l & 1) ? lw : 0.f)));
#pragma unroll
          for (int k = 0; k < 8; k++) {
            const float wa = asf(__builtin_amdgcn_readlane(asu(wn), 4 * k)), wb = asf(__builtin_amdgcn_readlane(asu(wn), 4 * k + 2));
            wk[k] = (l >> 5) ? wb : wa;
          }
        }
      }
      STAMP(9);
      float o = ov[0] * wk[0], o2 = ov[1] * wk[1];
#pragma unroll
      for (int k = 2; k < 8; k += 2) { o = fmaf(wk[k], ov[k], o); o2 = fmaf(wk[k + 1], ov[k + 1], o2); }
      o = x32_sum(o + o2);
      if (l < 32) *(LAS u16*)(L + L_OB + (h * 32 + l) * 2) = f2bf(o);
      wg_barrier();
    } else {
      const int hh = h - 2;
      if (WODEFER) cnt_wait(cnt + C_VISS, 8);
      const unsigned char* wsrc = (const unsigned char*)P.wo + (size_t)(r * 32) * 896;
#pragma unroll
      for (int d = 0; d < 5; d++) if (hh + 6 * d < 28) glds16(wsrc + (hh + 6 * d) * 1024, l * 16, lds0 + L_K + (hh + 6 * d) * 1024);
      waitvm<0>();
      cnt_add(cnt0 + 4 * C_W);
      cnt_wait(cnt + C_W, 6);
      u32x4 wf[5];
#pragma unroll
      for (int t = 0; t < 5; t++) {
        const int tile = hh + 6 * t < 28 ? hh + 6 * t : hh;
        const u32 a = lds0 + L_K + (8 * g + q4) * 896 + tile * 32 + 8 * p4;
        wf[t] = tr_frag(a, a + 4 * 896);
      }
      gu64* dst = blk + (g & 1) * 448 + 16 * (hh + 6 * (g >> 1)) + n;
      const LAS u32x4* ap = (const LAS u32x4*)(L + L_OB + (n & 1) * 64 + g * 16);
      asm volatile("" : "+v"(dst), "+v"(ap));
      STAMP(8);
      wg_barrier();
      const u32x4 af = *ap;
      f32x4 d[5];
#pragma unroll
      for (int t = 0; t < 5; t++) d[t] = mfma16(af, wf[t], f32x4{0.f, 0.f, 0.f, 0.f});
#pragma unroll
      for (int a = 0; a < 3; a++) {
        const int t1 = 2 * a + 1 < 5 ? 2 * a + 1 : 2 * a;
        const float va = (g & 1) ? d[2 * a][1] : d[2 * a][0], vb2 = (g & 1) ? d[t1][1] : d[t1][0];
        if (a < 2 || (g < 2 && hh < 4)) put(dst + a * 192, (g >> 1) ? vb2 : va, tgy);
      }
      STAMP(10);
    }
  } else if (idx < 24) {
    if (xcd == 7 || h == 7) { FLUSH(); return; }
    const int fs = xcd * 8 + (idx - 16);
    if (WDEFER == 1) cnt_wait(cnt + C_VISS, 8);
    if (WDEFER == 2) cnt_wait(cnt + C_DONE, 8);
    const unsigned char* w1s = (const unsigned char*)P.w1 + fs * 64;
    const unsigned char* w2s = (const unsigned char*)P.w2 + (size_t)fs * 32 * 896;
#pragma unroll
    for (int d = 0; d < 4; d++) glds16(w1s + (size_t)(h * 4 + d) * 16 * 3584, (l >> 2) * 3584 + (l & 3) * 16, lds0 + L_K + (h * 4 + d) * 1024);
#pragma unroll
    for (int d = 0; d < 4; d++) glds16(w2s + (h * 4 + d) * 1024, l * 16, lds0 + L_K + 28672 + (h * 4 + d) * 1024);
    waitvm<0>();
    cnt_add(cnt0 + 4 * C_W);
    cnt_wait(cnt + C_W, 7);
    u32x4 w1f[2][2], w2f[4];
#pragma unroll
    for (int a = 0; a < 2; a++)
#pragma unroll
      for (int t = 0; t < 2; t++) {
        const u32 ad = lds0 + L_K + (32 * (2 * h + a) + 8 * g + q4) * 64 + 32 * t + 8 * p4;
        w1f[a][t] = tr_frag(ad, ad + 4 * 64);
      }
#pragma unroll
    for (int t = 0; t < 4; t++) {
      const u32 a = lds0 + L_K + 28672 + (8 * g + q4) * 896 + (64 * h + 16 * t) * 2 + 8 * p4;
      w2f[t] = tr_frag(a, a + 4 * 896);
    }
    const u32x4 ones = {0x3f803f80u, 0x3f803f80u, 0x3f803f80u, 0x3f803f80u};
    float cs;
    {
      f32x4 c0 = mfma16(ones, w1f[0][0], f32x4{0.f, 0.f, 0.f, 0.f}), c1 = mfma16(ones, w1f[0][1], f32x4{0.f, 0.f, 0.f, 0.f});
      c0 = mfma16(ones, w1f[1][0], c0); c1 = mfma16(ones, w1f[1][1], c1);
      if (g < 2) *(LAS float*)(L + L_UR + h * 320 + (16 * g + n) * 4) = g ? c1[0] : c0[0];
      wg_barrier();
      cs = 0.f;
#pragma unroll
      for (int k = 0; k < 7; k++) cs += *(LAS float*)(L + L_UR + k * 320 + (l & 31) * 4);
      wg_barrier();
    }
    const int oi = (g & 1) * 448 + 64 * h + 16 * (g >> 1) + n;
    gu64* dst = ws + G_P1 + fs * 1024 + oi;
    const gu64* hp = ws + G_H + fs * 512 + (n & 1) * 224 + 32 * h + 4 * g;
    asm volatile("" : "+v"(dst), "+v"(hp), "+v"(cs));
    STAMP(13);
    u32x4 af[2];
    for (int spins = 0; spins < 4 * SPIN_CAP; spins++) {
      u64 gg[8];
      u32 bad = 0;
#pragma unroll
      for (int k = 0; k < 8; k++) gg[k] = __hip_atomic_load(hp + (k >> 2) * 16 + (k & 3), RLX_AGENT);
#pragma unroll
      for (int k = 0; k < 8; k++) { bad |= (u32)(gg[k] >> 32) ^ tg.hi; af[k >> 2][k & 3] = (u32)gg[k]; }
      if (__all(bad == 0u)) break;
    }
    STAMP(14);
    f32x4 u0, u1, st;
    {
      const f32x4 z = {0.f, 0.f, 0.f, 0.f};
      const u32x4 b0 = n < 2 ? af[0] : ones, b1 = n < 2 ? af[1] : ones;
      u0 = mfma16(af[0], w1f[0][0], z); u1 = mfma16(af[0], w1f[0][1], z); st = mfma16(af[0], b0, z);
      u0 = mfma16(af[1], w1f[1][0], u0); u1 = mfma16(af[1], w1f[1][1], u1); st = mfma16(af[1], b1, st);
    }
    {
      const float uv = (g >> 1) ? ((g & 1) ? u1[1] : u1[0]) : ((g & 1) ? u0[1] : u0[0]);
      *(LAS float*)(L + L_UR + h * 320 + ((g & 1) * 32 + (g >> 1) * 16 + n) * 4) = uv;
      if (l < 4) *(LAS float*)(L + L_UR + h * 320 + (64 + (l & 1) * 2 + (l >> 1)) * 4) = (l & 1) ? st[1] : st[0];
    }
    wg_barrier();
    {
      float ua = 0.f;
      f32x2 sa = {0.f, 0.f};
#pragma unroll
      for (int k = 0; k < 7; k++) {
        ua += *(LAS float*)(L + L_UR + k * 320 + l * 4);
        sa += *(LAS f32x2*)(L + L_UR + k * 320 + 256 + (l >> 5) * 8);
      }
      const float mean = sa[1] * (1.f / 448.f);
      const float var = rbf(fmaxf(fmaf(-mean, mean, sa[0] * (1.f / 448.f)), 0.f));
      const float rden = __builtin_amdgcn_rcpf(rbf(__builtin_amdgcn_sqrtf(rbf(var + 1e-5f))));
      const float u = rbf(fmaf(-rbf(mean), cs, ua) * rden);
      const float f = u * __builtin_amdgcn_rcpf(1.f + __builtin_amdgcn_exp2f(-u * 1.4426950408889634f));
      *(LAS u16*)(L + L_FB + h * 128 + l * 2) = f2bf(f);
    }
    waitlgkm();
    const u32x4 ff = *(LAS u32x4*)(L + L_FB + h * 128 + (n & 1) * 64 + g * 16);
    f32x4 d[4];
#pragma unroll
    for (int t = 0; t < 4; t++) d[t] = mfma16(ff, w2f[t], f32x4{0.f, 0.f, 0.f, 0.f});
#pragma unroll
    for (int a = 0; a < 2; a++) {
      const float va = (g & 1) ? d[2 * a][1] : d[2 * a][0], vb2 = (g & 1) ? d[2 * a + 1][1] : d[2 * a + 1][0];
      put(dst + a * 32, (g >> 1) ? vb2 : va, tgo);
    }
    STAMP(15);
  } else if (idx < 31) {
    const int t = (idx - 24) * 8 + xcd;
    float xv = bflo((u32)P.x[16 * t + n]);
    const gu64* src = ws + G_P1 + (16 * h + g) * 1024 + 16 * t + n;
    gu64* hdst = ws + G_H + (7 * h + (l >> 3)) * 512 + 8 * t + (l & 7);
    asm volatile("" : "+v"(xv), "+v"(hdst), "+v"(src));
    cnt_wait(cnt + C_DONE, 8);
    slow_start(src, tgy);
    float yv[4];
    sweep<4>(src, 4096, tgy, yv);
    STAMP(16);
    {
      const float y = x32_sum(x16_sum((yv[0] + yv[1]) + (yv[2] + yv[3])));
      if (l < 16) *(LAS float*)(L + L_UR + h * 64 + n * 4) = y;
    }
    wg_barrier();
    float hv;
    {
      float y = 0.f;
#pragma unroll
      for (int k = 0; k < 8; k++) y += *(LAS float*)(L + L_UR + k * 64 + n * 4);
      hv = rbf(xv + rbf(y));
      const u32 lo = __builtin_amdgcn_ds_bpermute(8 * (l & 7), asu(hv)), hi = __builtin_amdgcn_ds_bpermute(8 * (l & 7) + 4, asu(hv));
      if (l < 56) __hip_atomic_store(hdst, ((u64)tg.hi << 32) | (u64)((lo >> 16) | hi), RLX_AGENT);
    }
    STAMP(17);
    if (h < 7) {
      const gu64* src2 = ws + G_P1 + (8 * h + g) * 1024 + 16 * t + n;
      slow_start(src2, tgo);
      float ov[2];
      sweep<2>(src2, 4096, tgo, ov);
      STAMP(11);
      const float y = x32_sum(x16_sum(ov[0] + ov[1]));
      if (l < 16) *(LAS float*)(L + L_UR + 512 + h * 64 + n * 4) = y;
    }
    wg_barrier();
    if (h == 0) {
      float y = 0.f;
#pragma unroll
      for (int k = 0; k < 7; k++) y += *(LAS float*)(L + L_UR + 512 + k * 64 + n * 4);
      if (l < 16) P.out[16 * t + n] = f2bf(hv + rbf(y));
      if (t == 0 && l == 0) __hip_atomic_store(ws + G_EPOCH, *(LAS u64*)(L + L_TAG + 8), RLX_AGENT);
      STAMP(12);
    }
  }
  FLUSH();
}

extern "C" void kernel_launch(void* const* d_in, const int* in_sizes, int n_in, void* d_out, int out_size, void* d_ws, size_t ws_size,
                              hipStream_t stream) {
  Params p;
  p.x = (const u16*)d_in[0]; p.kc = (const u16*)d_in[1]; p.vc = (const u16*)d_in[2]; p.wq = (const u16*)d_in[3]; p.wo = (const u16*)d_in[4];
  p.w1 = (const u16*)d_in[6]; p.w2 = (const u16*)d_in[7]; p.out = (u16*)d_out; p.ws = (u64*)d_ws;
  fused_block_kernel<<<256, NT, 0, stream>>>(p);
}
```

```cpp
#define PRETOUCH 0
#include <hip/hip_runtime.h>
#include <stdint.h>

typedef unsigned short u16;
typedef unsigned u32;
typedef unsigned long long u64;
typedef u32 u32x4 __attribute__((ext_vector_type(4)));
typedef u32 u32x2 __attribute__((ext_vector_type(2)));
typedef float f32x4 __attribute__((ext_vector_type(4)));
typedef float f32x2 __attribute__((ext_vector_type(2)));
typedef __bf16 bf16x8 __attribute__((ext_vector_type(8)));
typedef __bf16 bf16x2 __attribute__((ext_vector_type(2)));
#define GAS __attribute__((address_space(1)))
#define LAS __attribute__((address_space(3)))
typedef GAS u64 gu64;
#define RLX_AGENT __ATOMIC_RELAXED, __HIP_MEMORY_SCOPE_AGENT
#define RLX_WG __ATOMIC_RELAXED, __HIP_MEMORY_SCOPE_WORKGROUP

#define WIN 4
#define WDEFER 2
#define WODEFER 1
#define QTRIG 6
#define OPENW 1
#define XCDMAP 0
#define SLOWS 4
#define NOSTREAM 0
#define FSLOW 0
#define FPV 2
#define YREP 1
#define WAT 8
#define G_BASE 0
static_assert(QTRIG + WIN - 2 < 16, "trigger inside the K loop");

constexpr int NT = 1024;
constexpr int SPIN_CAP = 1 << 16;
constexpr int G_EPOCH = 0;
constexpr int G_QP = 512;
constexpr int G_P1 = G_QP + 32768;
constexpr int G_S1 = G_P1 + 131072;
constexpr int G_H = G_S1 + 1024;
constexpr int G_Y = G_H + 512;
constexpr int G_END = G_Y + YREP * 32 * 512;
constexpr int G_STAMP = 1 << 20;
constexpr int L_K = 0;
constexpr int L_RED = 65536;
constexpr int L_WQ = 131072;
constexpr int L_XN = L_WQ + 14336;
constexpr int L_Q = L_XN + 512;
constexpr int L_P = L_Q + 1024;
constexpr int L_SM = L_P + 1024;
constexpr int L_CNT = L_SM + 128;
constexpr int L_TAG = L_CNT + 64;
constexpr int L_OB = L_TAG + 16;
constexpr int L_UR = L_OB + 512;
constexpr int L_FB = L_UR + 8 * 320;
constexpr int L_END = L_FB + 1024;
enum { C_Q = 0, C_K, C_WQ, C_KT, C_MAX, C_P, C_VISS, C_DONE, C_W, C_PV, C_N };

struct Params {
  const u16* x; const u16* kc; const u16* vc; const u16* wq; const u16* wo; const u16* w1; const u16* w2; u16* out; u64* ws;
};

__device__ __forceinline__ float asf(u32 u) { return __builtin_bit_cast(float, u); }
__device__ __forceinline__ u32 asu(float f) { return __builtin_bit_cast(u32, f); }
__device__ __forceinline__ float bflo(u32 u) { return asf(u << 16); }
__device__ __forceinline__ float bfhi(u32 u) { return asf(u & 0xffff0000u); }
__device__ __forceinline__ u16 f2bf(float f) { return __builtin_bit_cast(u16, (__bf16)f); }
__device__ __forceinline__ float rbf(float f) { return asf((u32)f2bf(f) << 16); }
__device__ __forceinline__ u32 pk2bf(float lo, float hi) { bf16x2 v = {(__bf16)lo, (__bf16)hi}; return __builtin_bit_cast(u32, v); }
template <int CTRL> __device__ __forceinline__ float dpp(float x) {
  return __builtin_bit_cast(float, __builtin_amdgcn_mov_dpp(__builtin_bit_cast(int, x), CTRL, 0xf, 0xf, true));
}
__device__ __forceinline__ float x16_sum(float x) { auto s = __builtin_amdgcn_permlane16_swap(asu(x), asu(x), false, false); return asf(s[0]) + asf(s[1]); }
__device__ __forceinline__ float x32_sum(float x) { auto s = __builtin_amdgcn_permlane32_swap(asu(x), asu(x), false, false); return asf(s[0]) + asf(s[1]); }
__device__ __forceinline__ float x16_max(float x) { auto s = __builtin_amdgcn_permlane16_swap(asu(x), asu(x), false, false); return fmaxf(asf(s[0]), asf(s[1])); }
__device__ __forceinline__ float row_sum(float x) { x += dpp<0xB1>(x); x += dpp<0x4E>(x); x += dpp<0x141>(x); x += dpp<0x128>(x); return x; }
__device__ __forceinline__ float row_max(float x) {
  x = fmaxf(x, dpp<0xB1>(x)); x = fmaxf(x, dpp<0x4E>(x)); x = fmaxf(x, dpp<0x141>(x)); x = fmaxf(x, dpp<0x128>(x)); return x;
}
__device__ __forceinline__ float wave_sum(float x) { return x32_sum(x16_sum(row_sum(x))); }
__device__ __forceinline__ f32x4 mfma16(u32x4 a, u32x4 b, f32x4 c) {
  return __builtin_amdgcn_mfma_f32_16x16x32_bf16(__builtin_bit_cast(bf16x8, a), __builtin_bit_cast(bf16x8, b), c, 0, 0, 0);
}
template <int N> __device__ __forceinline__ void waitvm() { asm volatile("s_waitcnt vmcnt(%0)" ::"i"(N) : "memory"); }
__device__ __forceinline__ void waitlgkm() { asm volatile("s_waitcnt lgkmcnt(0)" ::: "memory"); }
__device__ __forceinline__ void wg_barrier() { asm volatile("s_waitcnt lgkmcnt(0)\n\ts_barrier" ::: "memory"); }
#define POLKV " nt"
#define POLV POLKV
#define POLF " nt"
#define POLW " nt"
__device__ __forceinline__ void glds16(const void* sbase, u32 voff, u32 lds_dst) {
  u32 keep;
  asm volatile("s_mov_b32 %0, m0\n\ts_mov_b32 m0, %3\n\ts_nop 0\n\tglobal_load_lds_dwordx4 %1, %2" POLW "\n\ts_mov_b32 m0, %0"
               : "=&s"(keep) : "v"(voff), "s"(sbase), "s"(lds_dst) : "memory");
}
__device__ __forceinline__ void glds16k(const void* sbase, u32 voff, u32 lds_dst) {
  u32 keep;
  asm volatile("s_mov_b32 %0, m0\n\ts_mov_b32 m0, %3\n\ts_nop 0\n\tglobal_load_lds_dwordx4 %1, %2" POLKV "\n\ts_mov_b32 m0, %0"
               : "=&s"(keep) : "v"(voff), "s"(sbase), "s"(lds_dst) : "memory");
}
__device__ __forceinline__ u32x4 tr_frag(u32 a0, u32 a1) {
  u32x2 r0, r1;
  asm volatile("ds_read_b64_tr_b16 %0, %2\n\tds_read_b64_tr_b16 %1, %3\n\ts_waitcnt lgkmcnt(0)" : "=&v"(r0), "=&v"(r1) : "v"(a0), "v"(a1) : "memory");
  return u32x4{r0.x, r0.y, r1.x, r1.y};
}

struct Tag { u32 hi, lo; };
__device__ __forceinline__ void put(gu64* p, float v, Tag t) {
  __hip_atomic_store(p, ((u64)t.hi << 32) | (u64)((asu(v) & 0xffffff00u) | t.lo), RLX_AGENT);
}
__device__ __forceinline__ u32 tag_bad(u64 g, Tag t) { return (((u32)(g >> 32)) ^ t.hi) | (((u32)g ^ t.lo) & 0xffu); }
__device__ __forceinline__ float dec(u64 g) { return asf((u32)g & 0xffffff00u); }
template <int N> __device__ __forceinline__ void sweep(const gu64* p, int S, Tag t, float (&v)[N]) {
  for (int spins = 0; spins < SPIN_CAP; spins++) {
    u64 g[N];
    u32 bh = 0, bl = 0;
#pragma unroll
    for (int k = 0; k < N; k++) g[k] = __hip_atomic_load(p + k * S, RLX_AGENT);
#pragma unroll
    for (int k = 0; k < N; k++) { bh |= (u32)(g[k] >> 32) ^ t.hi; bl |= (u32)g[k] ^ t.lo; v[k] = dec(g[k]); }
    if (__all((bh | (bl & 0xffu)) == 0u)) break;
  }
}
__device__ __forceinline__ void slow_start(const gu64* p, Tag t) {
  for (int spins = 0; spins < SPIN_CAP; spins++) {
    if (__any(tag_bad(__hip_atomic_load(p, RLX_AGENT), t) == 0u)) break;
    __builtin_amdgcn_s_sleep(SLOWS);
  }
}

__device__ __forceinline__ void cnt_add(u32 addr) {
  u64 keep;
  asm volatile("s_waitcnt lgkmcnt(0)\n\ts_mov_b64 %0, exec\n\ts_mov_b64 exec, 1\n\tds_add_u32 %1, %2\n\ts_mov_b64 exec, %0"
               : "=&s"(keep) : "v"(addr), "v"(1u) : "memory");
}
__device__ __forceinline__ void cnt_wait(LAS u32* c, u32 n) {
  while (__hip_atomic_load(c, RLX_WG) < n) __builtin_amdgcn_s_sleep(1);
  asm volatile("" ::: "memory");
}
__device__ __forceinline__ void cnt_spin(LAS u32* c, u32 n) {
  while (__hip_atomic_load(c, RLX_WG) < n) {}
  asm volatile("" ::: "memory");
}

#define STAMP(i) do {} while (0)
#define FLUSH() do {} while (0)

__global__ void __launch_bounds__(NT, 4) fused_block_kernel(Params P) {
  __shared__ __attribute__((aligned(16))) unsigned char smem[L_END];
  LAS unsigned char* L = (LAS unsigned char*)smem;
  const u32 lds0 = (u32)(size_t)smem;
  const int wg = blockIdx.x;
  const int wave = __builtin_amdgcn_readfirstlane(threadIdx.x >> 6), l = threadIdx.x & 63;
  const int n = l & 15, g = l >> 4;
  const int xcd = wg & 7, idx = wg >> 3;
  const int kvh = (wg >> 4) & 7, b = wg >> 7, s = wg & 15;
  const int pair = b * 8 + kvh, aidx = b * 16 + s;
  gu64* ws = (gu64*)P.ws + G_BASE;
  LAS u32* cnt = (LAS u32*)(L + L_CNT);
  const u32 cnt0 = lds0 + L_CNT;

  if (wave < 8) {
    const int w = wave, hf = l >> 5, c = l & 31;
    if (threadIdx.x < 16) cnt[threadIdx.x] = 0u;
    STAMP(0);
    const unsigned char* kb = (const unsigned char*)P.kc + ((size_t)pair * 4096 + s * 256) * 512;
    const unsigned char* vb = (const unsigned char*)P.vc + ((size_t)pair * 4096 + s * 256) * 512;
    const int wr = (w + b) & 7;
    const u32 koff = hf * 512 + ((c ^ ((2 * wr + hf) & 15)) * 16);
    wg_barrier();
#pragma unroll
    for (int i = 0; i < 16; i++) {
      const int gc = ((i + s) & 15) * 8 + wr;
      glds16k(kb + gc * 1024, koff, lds0 + L_K + gc * 1024);
      if (i >= WIN - 1) waitvm<WIN - 1>();
      if (i == QTRIG + WIN - 2) cnt_add(cnt0 + 4 * C_KT);
    }
    u32x4 v[16];
    const u32 voff = g * 512 + n * 16;
    bool open = false;
#pragma unroll
    for (int i = 0; i < 16; i++) {
      const int gc2 = (((i >> 1) + s) & 7) * 8 + wr;
      asm volatile("global_load_dwordx4 %0, %1, %2 offset:%3" POLV : "=v"(v[i]) : "v"(voff), "s"(vb + gc2 * 2048), "i"((i & 1) * 256) : "memory");
      if (i < 15 && !open) waitvm<WIN - 1>();
      if (i == WIN - 2) cnt_add(cnt0 + 4 * C_K);
      if (OPENW && i >= WIN - 2 && i < 15 && !open) {
        const u64 qk = __hip_atomic_load((LAS u64*)(cnt + C_Q), RLX_WG);
        open = __builtin_amdgcn_readfirstlane((u32)qk + (u32)(qk >> 32)) >= 16;
      }
    }
    cnt_add(cnt0 + 4 * C_VISS);
    STAMP(1);
    cnt_wait(cnt + C_P, 8);
    STAMP(18);
    const u32x4 pf = *(LAS u32x4*)(L + L_P + (n >> 3) * 512 + wr * 64 + g * 16);
    const u32x4 zero4 = {0, 0, 0, 0};
    const u32x4 pa0 = (n & 4) ? zero4 : pf, pa1 = (n & 4) ? pf : zero4;
    const Tag tg{*(LAS u32*)(L + L_TAG), *(LAS u32*)(L + L_TAG + 4)};
    gu64* dst = ws + G_P1 + ((kvh * 16 + 2 * w + hf) * 2 + b) * 512 + s * 32 + c;
    asm volatile("" : "+v"(dst));
    u32x4 bf0[8], bf1[8];
#pragma unroll
    for (int t = 0; t < 4; t++) {
      asm volatile("s_waitcnt vmcnt(%4)" : "+v"(v[4 * t]), "+v"(v[4 * t + 1]), "+v"(v[4 * t + 2]), "+v"(v[4 * t + 3]) : "i"(12 - 4 * t) : "memory");
#pragma unroll
      for (int e = 0; e < 8; e++) {
        const u32 sel = (e & 1) ? 0x07060302u : 0x05040100u;
        bf0[e][t] = __builtin_amdgcn_perm(v[4 * t + 2][e >> 1], v[4 * t][e >> 1], sel);
        bf1[e][t] = __builtin_amdgcn_perm(v[4 * t + 3][e >> 1], v[4 * t + 1][e >> 1], sel);
      }
    }
    f32x4 d[8];
#pragma unroll
    for (int e = 0; e < 8; e++) d[e] = mfma16(pa0, bf0[e], f32x4{0.f, 0.f, 0.f, 0.f});
#pragma unroll
    for (int e = 0; e < 8; e++) d[e] = mfma16(pa1, bf1[e], d[e]);
    *(LAS f32x4*)(L + L_RED + w * 2048 + l * 32) = f32x4{d[0][0], d[1][0], d[2][0], d[3][0]};
    *(LAS f32x4*)(L + L_RED + w * 2048 + l * 32 + 16) = f32x4{d[4][0], d[5][0], d[6][0], d[7][0]};
    cnt_add(cnt0 + 4 * C_PV);
    STAMP(19);
    float o = 0.f;
    for (int spins = 0; spins < (1 << 22); spins++) {
      u32 x[8];
      asm volatile("" ::: "memory");
#pragma unroll
      for (int ww = 0; ww < 8; ww++) x[ww] = *(volatile LAS u32*)(L + L_RED + ww * 2048 + (64 * w + l) * 4);
      const u32 mx = max(max(max(x[0], x[1]), max(x[2], x[3])), max(max(x[4], x[5]), max(x[6], x[7])));
      o = ((asf(x[0]) + asf(x[1])) + (asf(x[2]) + asf(x[3]))) + ((asf(x[4]) + asf(x[5])) + (asf(x[6]) + asf(x[7])));
      if (__all(mx != 0xffffffffu)) break;
    }
    put(dst, o, tg);
    cnt_add(cnt0 + 4 * C_DONE);
    STAMP(3);
    FLUSH();
    return;
  }

  const int h = wave - 8, q4 = n >> 2, p4 = n & 3;
  Tag tg;
  {
    const int cg = 8 * kvh + (aidx >> 2), kq = aidx & 3;
    const unsigned char* wqs = (const unsigned char*)P.wq + (size_t)(112 * kq) * 8192 + 128 * cg;
    if (h < 7) {
#pragma unroll
      for (int d = 0; d < 2; d++) glds16(wqs + (size_t)(16 * h + 8 * d) * 8192, (l >> 3) * 8192 + (l & 7) * 16, lds0 + L_WQ + (2 * h + d) * 1024);
    }
    u32x4 xr = {0, 0, 0, 0};
    if (h == 5 || h == 6) xr = *(const u32x4*)(P.x + (h & 1) * 448 + (l < 56 ? l : 55) * 8);
    if (h == 7) {
      u64 ep;
      u32 junk;
      const gu64* epp = ws + G_EPOCH;
      const unsigned char* tp = (const unsigned char*)P.out;
      const int rr = xcd * 16 + idx, fs = xcd * 8 + (idx - 16);
      if (l == 1) tp = (const unsigned char*)P.vc + ((size_t)pair * 4096 + s * 256) * 512;
      if (l == 2) tp = (const unsigned char*)P.vc + ((size_t)pair * 4096 + s * 256 + 255) * 512;
      if (l == 3) tp = (const unsigned char*)P.kc + ((size_t)pair * 4096 + s * 256 + 255) * 512;
      if (l == 4) tp = (const unsigned char*)(ws + G_P1 + kvh * 16384);
      if (l == 5) tp = (const unsigned char*)(ws + G_P1 + 131071);
      if (l == 6) tp = (const unsigned char*)(ws + G_END - 1);
      if (idx < 16) {
        if (l == 7) tp = (const unsigned char*)P.wo + (size_t)rr * 32 * 896;
        if (l == 8) tp = (const unsigned char*)P.wo + (size_t)rr * 32 * 896 + 28668;
      } else if (idx < 24 && xcd < 7) {
        if (l == 7) tp = (const unsigned char*)P.w1 + fs * 64;
        if (l == 8) tp = (const unsigned char*)P.w1 + (size_t)447 * 3584 + fs * 64;
        if (l == 9) tp = (const unsigned char*)P.w1 + (size_t)224 * 3584 + fs * 64;
        if (l == 10) tp = (const unsigned char*)P.w2 + (size_t)fs * 32 * 896;
        if (l == 11) tp = (const unsigned char*)P.w2 + (size_t)fs * 32 * 896 + 28668;
      }
      asm volatile("global_load_dwordx2 %0, %1, off sc1" : "=v"(ep) : "v"(epp) : "memory");
      wg_barrier();
      asm volatile("s_waitcnt vmcnt(0)" : "+v"(ep) :: "memory");
      const u64 ne = ep * 6364136223846793005ull + 1442695040888963407ull;
      if (l == 0) *(LAS u32x4*)(L + L_TAG) = u32x4{(u32)(ne >> 8), (u32)ne & 0xffu, (u32)ne, (u32)(ne >> 32)};
      cnt_add(cnt0 + 4 * C_WQ);
    } else {
      wg_barrier();
      if (h >= 5) {
        float xv[8];
#pragma unroll
        for (int k = 0; k < 4; k++) { xv[2 * k] = bflo(xr[k]); xv[2 * k + 1] = bfhi(xr[k]); }
        float sm = ((xv[0] + xv[1]) + (xv[2] + xv[3])) + ((xv[4] + xv[5]) + (xv[6] + xv[7]));
        const float mean = wave_sum(l < 56 ? sm : 0.f) * (1.f / 448.f);
        float ss = 0.f;
#pragma unroll
        for (int k = 0; k < 8; k++) { const float d = xv[k] - mean; ss = fmaf(d, d, ss); }
        const float var = rbf(wave_sum(l < 56 ? ss : 0.f) * (1.f / 448.f)), mb = rbf(mean);
        const float rden = __builtin_amdgcn_rcpf(rbf(__builtin_amdgcn_sqrtf(rbf(var + 1e-5f))));
        u32x4 o;
#pragma unroll
        for (int k = 0; k < 4; k++) o[k] = pk2bf(rbf(xv[2 * k] - mb) * rden, rbf(xv[2 * k + 1] - mb) * rden);
        const int rl = l - 14 * kq;
        if (rl >= 0 && rl < 14) *(LAS u32x4*)(L + L_XN + (h & 1) * 256 + rl * 16) = o;
        if (l >= 62) *(LAS u32x4*)(L + L_XN + (h & 1) * 256 + (l - 48) * 16) = u32x4{0, 0, 0, 0};
      }
      waitvm<0>();
      cnt_add(cnt0 + 4 * C_WQ);
    }
    cnt_spin(cnt + C_WQ, 8);
    tg.hi = __builtin_amdgcn_readfirstlane(*(LAS u32*)(L + L_TAG));
    tg.lo = __builtin_amdgcn_readfirstlane(*(LAS u32*)(L + L_TAG + 4));
    if (h < 4) {
      f32x4 acc = {0.f, 0.f, 0.f, 0.f};
#pragma unroll
      for (int ks = 0; ks < 4; ks++) {
        const u32x4 af = *(LAS u32x4*)(L + L_XN + (n & 1) * 256 + ks * 64 + g * 16);
        int r0 = 32 * ks + 8 * g + q4;
        if (ks == 3) r0 = (g >= 2) ? r0 - 16 : r0;
        const u32 a = lds0 + L_WQ + r0 * 128 + 32 * h + 8 * p4;
        acc = mfma16(af, tr_frag(a, a + 4 * 128), acc);
      }
      if (g < 2) put(ws + G_QP + (g * 4 + kq) * 4096 + 64 * cg + 16 * h + n, g ? acc[1] : acc[0], tg);
      STAMP(4);
    }
    cnt_wait(cnt + C_KT, 8);
    float qg[4];
    sweep<4>(ws + G_QP + b * 4 * 4096 + kvh * 512 + 64 * h + l, 4096, tg, qg);
    *(LAS u16*)(L + L_Q + (64 * h + l) * 2) = f2bf((qg[0] + qg[1]) + (qg[2] + qg[3]));
    cnt_add(cnt0 + 4 * C_Q);
    STAMP(5);
  }
  {
    cnt_wait(cnt + C_Q, 8);
    u32x4 qf[8];
#pragma unroll
    for (int j = 0; j < 8; j++) qf[j] = *(LAS u32x4*)(L + L_Q + (n & 1) * 512 + j * 64 + g * 16);
    cnt_wait(cnt + C_K, 8);
    STAMP(6);
    const LAS unsigned char* kr = L + L_K + (32 * h + n) * 512;
    u32x4 kf[16];
#pragma unroll
    for (int j = 0; j < 8; j++) {
      kf[2 * j] = *(const LAS u32x4*)(kr + (((4 * j + g) ^ n) * 16));
      kf[2 * j + 1] = *(const LAS u32x4*)(kr + 8192 + (((4 * j + g) ^ n) * 16));
    }
    __builtin_amdgcn_sched_barrier(0);
    f32x4 acc0 = {0.f, 0.f, 0.f, 0.f}, acc1 = {0.f, 0.f, 0.f, 0.f};
#pragma unroll
    for (int j = 0; j < 8; j++) { acc0 = mfma16(qf[j], kf[2 * j], acc0); acc1 = mfma16(qf[j], kf[2 * j + 1], acc1); }
    const float sr = (g & 1) ? ((g >> 1) ? acc1[1] : acc1[0]) : ((g >> 1) ? acc0[1] : acc0[0]);
    const float sc = rbf(sr) * 0.0625f;
    const float mw = x16_max(row_max(sc));
    LAS float* sm = (LAS float*)(L + L_SM);
    if ((l & 31) == 0) sm[(g >> 1) * 8 + h] = mw;
    cnt_add(cnt0 + 4 * C_MAX);
    cnt_spin(cnt + C_MAX, 8);
    const f32x4 ma = *(LAS f32x4*)(sm + (g >> 1) * 8), mb = *(LAS f32x4*)(sm + (g >> 1) * 8 + 4);
    *(LAS u32x4*)(L + L_RED + h * 2048 + l * 32) = u32x4{~0u, ~0u, ~0u, ~0u};
    *(LAS u32x4*)(L + L_RED + h * 2048 + l * 32 + 16) = u32x4{~0u, ~0u, ~0u, ~0u};
    const float m = fmaxf(fmaxf(fmaxf(ma[0], ma[1]), fmaxf(ma[2], ma[3])), fmaxf(fmaxf(mb[0], mb[1]), fmaxf(mb[2], mb[3])));
    const u16 pb = f2bf(__builtin_amdgcn_exp2f((sc - m) * 1.4426950408889634f));
    const int row = 16 * (g & 1) + n;
    *(LAS u16*)(L + L_P + (g >> 1) * 512 + (row >> 2) * 64 + (row & 3) * 16 + ((h - s) & 7) * 2) = pb;
    const float lw = x16_sum(row_sum(bflo((u32)pb)));
    if ((l & 31) == 0) sm[16 + (g >> 1) * 8 + h] = lw;
    cnt_add(cnt0 + 4 * C_P);
    STAMP(7);
    if (h == 7) {
      cnt_spin(cnt + C_P, 8);
      const f32x4 la = *(LAS f32x4*)(sm + 16 + (g >> 1) * 8), lb = *(LAS f32x4*)(sm + 16 + (g >> 1) * 8 + 4);
      const float ls = ((la[0] + la[1]) + (la[2] + la[3])) + ((lb[0] + lb[1]) + (lb[2] + lb[3]));
      if ((l & 30) == 0) put(ws + G_S1 + (((kvh * 2 + (g >> 1)) * 2 + b) * 16 + s) * 2 + (l & 1), (l & 1) ? ls : m, tg);
    }
  }
  const Tag tgo{tg.hi, tg.lo ^ 2u};
  const u32 tgy = tg.hi ^ 0x5bd1e995u;
  const f32x4 z4 = {0.f, 0.f, 0.f, 0.f};
  constexpr u32 SLO = 0x05040100u, SHI = 0x07060302u;
  if (idx < 16) {
    const int r1 = (idx >> 2) * 8 + xcd, cq = idx & 3, cb = 2 * cq + h;
    const bool mm = h < 2 && cb < 7;
    u32x2 wr[4][8];
    cnt_wait(cnt + C_VISS, WAT);
    if (mm) {
      const unsigned char* wsrc = (const unsigned char*)P.wo + (size_t)(r1 * 128 + 8 * g) * 896 + 128 * cb + 8 * n;
#pragma unroll
      for (int ks = 0; ks < 4; ks++)
#pragma unroll
        for (int j = 0; j < 8; j++)
          asm volatile("global_load_dwordx2 %0, %1, off offset:%2" POLW : "=v"(wr[ks][j]) : "v"(wsrc + ks * 32 * 896 + (j >> 2) * 3584), "i"((j & 3) * 896) : "memory");
    }
    const gu64* pd = ws + G_P1 + (4 * r1 + (h >> 1)) * 1024 + (h & 1) * 512 + 2 * l;
    const gu64* ps = ws + G_S1 + ((r1 >> 1) * 2 + (h & 1)) * 32 + (l & 31);
    u32x4 wf[4][4];
    if (mm) {
#pragma unroll
      for (int ks = 0; ks < 4; ks++) {
        asm volatile("s_waitcnt vmcnt(%8)" : "+v"(wr[ks][0]), "+v"(wr[ks][1]), "+v"(wr[ks][2]), "+v"(wr[ks][3]), "+v"(wr[ks][4]), "+v"(wr[ks][5]),
                     "+v"(wr[ks][6]), "+v"(wr[ks][7]) : "i"(24 - 8 * ks) : "memory");
#pragma unroll
        for (int e = 0; e < 4; e++)
#pragma unroll
          for (int t = 0; t < 4; t++) wf[ks][e][t] = __builtin_amdgcn_perm(wr[ks][2 * t + 1][e >> 1], wr[ks][2 * t][e >> 1], (e & 1) ? SHI : SLO);
#pragma unroll
        for (int e = 0; e < 4; e++) asm volatile("" : "+v"(wf[ks][e]));
      }
    }
    GAS u32x4* dst = (GAS u32x4*)(ws + G_Y + r1 * 512 + (g & 1) * 224 + 32 * cb + 2 * n);
    const LAS u32x4* ap = (const LAS u32x4*)(L + L_OB + (n & 1) * 256 + g * 16);
    asm volatile("" : "+v"(dst), "+v"(ap));
    STAMP(8);
    cnt_wait(cnt + C_DONE, 8);
    STAMP(2);
    u32x4 pv[4];
    u64 sg;
    float wk[4];
    u32 pend = 0x1fu;
    for (int spins = 0; spins < 4 * SPIN_CAP && pend; spins++) {
#pragma unroll
      for (int k = 0; k < 4; k++) if (pend & (1u << k))
        asm volatile("global_load_dwordx4 %0, %1, off offset:%2 sc1" : "=v"(pv[k]) : "v"(pd), "i"(k * 1024) : "memory");
      if (pend & 0x10u) asm volatile("global_load_dwordx2 %0, %1, off sc1" : "=v"(sg) : "v"(ps) : "memory");
      asm volatile("s_waitcnt vmcnt(0)" : "+v"(pv[0]), "+v"(pv[1]), "+v"(pv[2]), "+v"(pv[3]), "+v"(sg) :: "memory");
#pragma unroll
      for (int k = 0; k < 4; k++) if (pend & (1u << k)) {
        if (__all(((pv[k][1] ^ tg.hi) | (pv[k][3] ^ tg.hi) | (((pv[k][0] ^ tg.lo) | (pv[k][2] ^ tg.lo)) & 0xffu)) == 0u)) pend &= ~(1u << k);
      }
      if ((pend & 0x10u) && __all(tag_bad(sg, tg) == 0u)) {
        pend &= 0xfu;
        const float st = dec(sg);
        const float M = x16_max(row_max((l & 1) ? -INFINITY : st));
        const float wgt = __builtin_amdgcn_exp2f((st - M) * 1.4426950408889634f);
        const float lw = dpp<0xB1>(wgt) * st;
        const float wn = wgt * __builtin_amdgcn_rcpf(x16_sum(row_sum((l & 1) ? lw : 0.f)));
#pragma unroll
        for (int k = 0; k < 4; k++) wk[k] = asf(__builtin_amdgcn_ds_bpermute(8 * (4 * k + g), asu(wn)));
      }
    }
    STAMP(9);
    {
      float o0 = 0.f, o1 = 0.f;
#pragma unroll
      for (int k = 0; k < 4; k++) { o0 = fmaf(wk[k], asf(pv[k][0] & 0xffffff00u), o0); o1 = fmaf(wk[k], asf(pv[k][2] & 0xffffff00u), o1); }
      o0 = x32_sum(x16_sum(o0)); o1 = x32_sum(x16_sum(o1));
      if (l < 16) *(LAS u32*)(L + L_OB + (h & 1) * 256 + (h >> 1) * 64 + n * 4) = pk2bf(o0, o1);
    }
    wg_barrier();
    if (mm) {
      u32x4 af[4];
#pragma unroll
      for (int ks = 0; ks < 4; ks++) af[ks] = ap[4 * ks];
      f32x4 d[4] = {z4, z4, z4, z4};
#pragma unroll
      for (int ks = 0; ks < 4; ks++)
#pragma unroll
        for (int e = 0; e < 4; e++) d[e] = mfma16(af[ks], wf[ks][e], d[e]);
      const u32 lo = (g & 1) ? pk2bf(d[0][1], d[1][1]) : pk2bf(d[0][0], d[1][0]);
      const u32 hi = (g & 1) ? pk2bf(d[2][1], d[3][1]) : pk2bf(d[2][0], d[3][0]);
      const u32x4 val = {lo, tgy, hi, tgy};
      if (YREP == 1) {
        if (g < 2) asm volatile("global_store_dwordx4 %0, %1, off sc1\n\ts_nop 1" :: "v"(dst), "v"(val) : "memory");
      } else {
#pragma unroll
        for (int c2 = 0; c2 < (YREP + 1) / 2; c2++)
          if (2 * c2 + 1 < YREP || g < 2)
            asm volatile("global_store_dwordx4 %0, %1, off sc1\n\ts_nop 1" :: "v"(dst + (2 * c2 + (g >> 1)) * (32 * 256)), "v"(val) : "memory");
      }
      STAMP(10);
    }
    if (h == 7 || cq < 2 || r1 >= 28) { FLUSH(); return; }
    const int t = 2 * r1 + cq - 2, tb = t >= 28 ? 1 : 0, col = 16 * t - 448 * tb;
    u32 hpk = 0;
    for (int spins = 0; spins < SPIN_CAP; spins++) {
      const u64 gg = __hip_atomic_load(ws + G_H + 8 * t + (n >> 1), RLX_AGENT);
      hpk = (u32)gg;
      if (__all((u32)(gg >> 32) == tgy)) break;
      __builtin_amdgcn_s_sleep(SLOWS);
    }
    STAMP(17);
    float fv[2];
    sweep<2>(ws + G_P1 + (tb * 56 + 8 * h + g) * 1024 + 512 + col + n, 4096, tgo, fv);
    STAMP(11);
    const float y = x32_sum(x16_sum(fv[0] + fv[1]));
    if (l < 16) *(LAS float*)(L + L_UR + h * 64 + n * 4) = y;
    wg_barrier();
    if (h == 0) {
      float yy = 0.f;
#pragma unroll
      for (int k = 0; k < 7; k++) yy += *(LAS float*)(L + L_UR + k * 64 + n * 4);
      if (l < 16) P.out[16 * t + n] = f2bf(((n & 1) ? bfhi(hpk) : bflo(hpk)) + rbf(yy));
      if (t == 0 && l == 0) __hip_atomic_store(ws + G_EPOCH, *(LAS u64*)(L + L_TAG + 8), RLX_AGENT);
      STAMP(12);
    }
  } else if (xcd < 7) {
    if (h == 7) { FLUSH(); return; }
    const int fs = xcd * 8 + ((idx - 16) >> 1), fb = idx & 1;
    u32 w1r[2][8];
    u32x2 w2r[8];
    cnt_wait(cnt + C_VISS, WAT);
    if (WDEFER == 2) cnt_wait(cnt + C_DONE, 8);
    if (WDEFER == 3) cnt_wait(cnt + C_PV, FPV);
    {
      const unsigned char* s1 = (const unsigned char*)P.w1 + (size_t)(64 * h + 8 * g) * 3584 + 64 * fs + 4 * n;
      const unsigned char* s2 = (const unsigned char*)P.w2 + (size_t)(32 * fs + 8 * g) * 896 + 128 * h + 8 * n;
#pragma unroll
      for (int a = 0; a < 2; a++)
#pragma unroll
        for (int j = 0; j < 8; j++) asm volatile("global_load_dword %0, %1, off" POLF : "=v"(w1r[a][j]) : "v"(s1 + (32 * a + j) * 3584) : "memory");
#pragma unroll
      for (int j = 0; j < 8; j++)
        asm volatile("global_load_dwordx2 %0, %1, off offset:%2" POLF : "=v"(w2r[j]) : "v"(s2 + (j >> 2) * 3584), "i"((j & 3) * 896) : "memory");
    }
    const u32x2 xw = *(const u32x2*)(P.x + fb * 448 + 64 * h + 4 * n);
    asm volatile("s_waitcnt vmcnt(0)" : "+v"(w1r[0][0]), "+v"(w1r[0][1]), "+v"(w1r[0][2]), "+v"(w1r[0][3]), "+v"(w1r[0][4]), "+v"(w1r[0][5]), "+v"(w1r[0][6]),
                 "+v"(w1r[0][7]), "+v"(w1r[1][0]), "+v"(w1r[1][1]), "+v"(w1r[1][2]), "+v"(w1r[1][3]), "+v"(w1r[1][4]), "+v"(w1r[1][5]), "+v"(w1r[1][6]),
                 "+v"(w1r[1][7]) :: "memory");
    asm volatile("" : "+v"(w2r[0]), "+v"(w2r[1]), "+v"(w2r[2]), "+v"(w2r[3]), "+v"(w2r[4]), "+v"(w2r[5]), "+v"(w2r[6]), "+v"(w2r[7]) :: "memory");
    u32x4 w1f[2][2], w2f[4];
#pragma unroll
    for (int t = 0; t < 4; t++) {
#pragma unroll
      for (int a = 0; a < 2; a++) {
        w1f[a][0][t] = __builtin_amdgcn_perm(w1r[a][2 * t + 1], w1r[a][2 * t], SLO);
        w1f[a][1][t] = __builtin_amdgcn_perm(w1r[a][2 * t + 1], w1r[a][2 * t], SHI);
      }
#pragma unroll
      for (int e = 0; e < 4; e++) w2f[e][t] = __builtin_amdgcn_perm(w2r[2 * t + 1][e >> 1], w2r[2 * t][e >> 1], (e & 1) ? SHI : SLO);
    }
    const u32x4 ones = {0x3f803f80u, 0x3f803f80u, 0x3f803f80u, 0x3f803f80u};
    float cs;
    {
      f32x4 c0 = mfma16(ones, w1f[0][0], z4), c1 = mfma16(ones, w1f[0][1], z4);
      c0 = mfma16(ones, w1f[1][0], c0); c1 = mfma16(ones, w1f[1][1], c1);
      if (g < 2) *(LAS float*)(L + L_UR + h * 160 + (2 * n + g) * 4) = g ? c1[0] : c0[0];
      wg_barrier();
      cs = 0.f;
#pragma unroll
      for (int k = 0; k < 7; k++) cs += *(LAS float*)(L + L_UR + k * 160 + (l & 31) * 4);
      wg_barrier();
    }
    gu64* dst = ws + G_P1 + (fb * 56 + fs) * 1024 + 512 + 64 * h + 4 * n + g;
    const gu64* src = ws + G_Y + (xcd % YREP) * (32 * 512) + g * 512 + fb * 224 + 32 * h + 2 * n;
    asm volatile("" : "+v"(dst), "+v"(src), "+v"(cs));
#pragma unroll
    for (int e = 0; e < 4; e++) asm volatile("" : "+v"(w2f[e]));
    asm volatile("" : "+v"(w1f[0][0]), "+v"(w1f[0][1]), "+v"(w1f[1][0]), "+v"(w1f[1][1]));
    STAMP(13);
    for (int spins = 0; FSLOW && spins < SPIN_CAP; spins++) {
      if (__any((u32)(__hip_atomic_load(src + h * 2048, RLX_AGENT) >> 32) == tgy)) break;
      __builtin_amdgcn_s_sleep(SLOWS);
    }
    u32x4 yv[8];
    {
      u32 pend = 0xffu;
      for (int spins = 0; spins < 4 * SPIN_CAP && pend; spins++) {
#pragma unroll
        for (int k = 0; k < 8; k++) if (pend & (1u << k))
          asm volatile("global_load_dwordx4 %0, %1, off sc1" : "=v"(yv[k]) : "v"(src + k * 2048) : "memory");
        asm volatile("s_waitcnt vmcnt(0)" : "+v"(yv[0]), "+v"(yv[1]), "+v"(yv[2]), "+v"(yv[3]), "+v"(yv[4]), "+v"(yv[5]), "+v"(yv[6]), "+v"(yv[7]) :: "memory");
#pragma unroll
        for (int k = 0; k < 8; k++) if (pend & (1u << k)) {
          if (__all(((yv[k][1] ^ tgy) | (yv[k][3] ^ tgy)) == 0u)) pend &= ~(1u << k);
        }
      }
    }
    STAMP(16);
    u32x2 hp;
    {
      float y[4] = {0.f, 0.f, 0.f, 0.f};
#pragma unroll
      for (int k = 0; k < 8; k++) { y[0] += bflo(yv[k][0]); y[1] += bfhi(yv[k][0]); y[2] += bflo(yv[k][2]); y[3] += bfhi(yv[k][2]); }
#pragma unroll
      for (int e = 0; e < 4; e++) y[e] = x32_sum(x16_sum(y[e]));
      hp.x = pk2bf(bflo(xw.x) + rbf(y[0]), bfhi(xw.x) + rbf(y[1]));
      hp.y = pk2bf(bflo(xw.y) + rbf(y[2]), bfhi(xw.y) + rbf(y[3]));
      if (l < 16) *(LAS u32x2*)(L + L_FB + h * 128 + n * 8) = hp;
    }
    waitlgkm();
    const u32x4 af0 = *(LAS u32x4*)(L + L_FB + h * 128 + g * 16), af1 = *(LAS u32x4*)(L + L_FB + h * 128 + 64 + g * 16);
    if (fs == 0 && l < 32) __hip_atomic_store(ws + G_H + fb * 224 + 32 * h + 2 * n + g, ((u64)tgy << 32) | (g ? hp.y : hp.x), RLX_AGENT);
    STAMP(14);
    f32x4 u0, u1, st;
    {
      const u32x4 b0 = n == 0 ? af0 : ones, b1 = n == 0 ? af1 : ones;
      u0 = mfma16(af0, w1f[0][0], z4); u1 = mfma16(af0, w1f[0][1], z4); st = mfma16(af0, b0, z4);
      u0 = mfma16(af1, w1f[1][0], u0); u1 = mfma16(af1, w1f[1][1], u1); st = mfma16(af1, b1, st);
    }
    {
      const float uv = (g == 0) ? u0[0] : (g == 1) ? u1[0] : st[0];
      if (g < 2 || l < 34) *(LAS float*)(L + L_UR + h * 160 + (g < 2 ? 2 * n + g : 32 + n) * 4) = uv;
    }
    wg_barrier();
    {
      float ua = 0.f;
      f32x2 sa = {0.f, 0.f};
#pragma unroll
      for (int k = 0; k < 7; k++) {
        ua += *(LAS float*)(L + L_UR + k * 160 + (l & 31) * 4);
        sa += *(LAS f32x2*)(L + L_UR + k * 160 + 128);
      }
      const float mean = sa[1] * (1.f / 448.f);
      const float var = rbf(fmaxf(fmaf(-mean, mean, sa[0] * (1.f / 448.f)), 0.f));
      const float rden = __builtin_amdgcn_rcpf(rbf(__builtin_amdgcn_sqrtf(rbf(var + 1e-5f))));
      const float u = rbf(fmaf(-rbf(mean), cs, ua) * rden);
      const float f = u * __builtin_amdgcn_rcpf(1.f + __builtin_amdgcn_exp2f(-u * 1.4426950408889634f));
      if (l < 32) *(LAS u16*)(L + L_FB + h * 128 + l * 2) = f2bf(f);
    }
    waitlgkm();
    const u32x4 ff = *(LAS u32x4*)(L + L_FB + h * 128 + g * 16);
    f32x4 d[4];
#pragma unroll
    for (int e = 0; e < 4; e++) d[e] = mfma16(ff, w2f[e], z4);
    put(dst, (g >> 1) ? ((g & 1) ? d[3][0] : d[2][0]) : ((g & 1) ? d[1][0] : d[0][0]), tgo);
    STAMP(15);
  }
  FLUSH();
}

extern "C" void kernel_launch(void* const* d_in, const int* in_sizes, int n_in, void* d_out, int out_size, void* d_ws, size_t ws_size,
                              hipStream_t stream) {
  Params p;
  p.x = (const u16*)d_in[0]; p.kc = (const u16*)d_in[1]; p.vc = (const u16*)d_in[2]; p.wq = (const u16*)d_in[3]; p.wo = (const u16*)d_in[4];
  p.w1 = (const u16*)d_in[6]; p.w2 = (const u16*)d_in[7]; p.out = (u16*)d_out; p.ws = (u64*)d_ws;
  fused_block_kernel<<<256, NT, 0, stream>>>(p);
}
```

```cpp
#define DIAGMODE 0
#define PRETOUCH 0
#include <hip/hip_runtime.h>
#include <stdint.h>

typedef unsigned short u16;
typedef unsigned u32;
typedef unsigned long long u64;
typedef u32 u32x4 __attribute__((ext_vector_type(4)));
typedef u32 u32x2 __attribute__((ext_vector_type(2)));
typedef float f32x4 __attribute__((ext_vector_type(4)));
typedef float f32x2 __attribute__((ext_vector_type(2)));
typedef __bf16 bf16x8 __attribute__((ext_vector_type(8)));
typedef __bf16 bf16x2 __attribute__((ext_vector_type(2)));
#define GAS __attribute__((address_space(1)))
#define LAS __attribute__((address_space(3)))
typedef GAS u64 gu64;
#define RLX_AGENT __ATOMIC_RELAXED, __HIP_MEMORY_SCOPE_AGENT
#define RLX_WG __ATOMIC_RELAXED, __HIP_MEMORY_SCOPE_WORKGROUP

#define WIN 4
#define WDEFER 2
#define WODEFER 1
#define QTRIG 6
#define OPENW 1
#define XCDMAP 0
#define SLOWS 4
#define NOSTREAM 0
#define FSLOW 0
#define FPV 2
#define YREP 1
#define WAT 8
#define G_BASE 0
static_assert(QTRIG + WIN - 2 < 16, "trigger inside the K loop");

constexpr int NT = 1024;
constexpr int SPIN_CAP = 1 << 16;
constexpr int G_EPOCH = 0;
constexpr int G_QP = 512;
constexpr int G_P1 = G_QP + 32768;
constexpr int G_S1 = G_P1 + 131072;
constexpr int G_H = G_S1 + 1024;
constexpr int G_Y = G_H + 512;
constexpr int G_END = G_Y + YREP * 32 * 512;
constexpr int G_STAMP = 1 << 20;
constexpr int L_K = 0;
constexpr int L_RED = 65536;
constexpr int L_WQ = 131072;
constexpr int L_XN = L_WQ + 14336;
constexpr int L_Q = L_XN + 512;
constexpr int L_P = L_Q + 1024;
constexpr int L_SM = L_P + 1024;
constexpr int L_CNT = L_SM + 128;
constexpr int L_TAG = L_CNT + 64;
constexpr int L_OB = L_TAG + 16;
constexpr int L_UR = L_OB + 512;
constexpr int L_FB = L_UR + 8 * 320;
constexpr int L_END = L_FB + 1024;
enum { C_Q = 0, C_K, C_WQ, C_KT, C_MAX, C_P, C_VISS, C_DONE, C_W, C_PV, C_N };

struct Params {
  const u16* x; const u16* kc; const u16* vc; const u16* wq; const u16* wo; const u16* w1; const u16* w2; u16* out; u64* ws;
};

__device__ __forceinline__ float asf(u32 u) { return __builtin_bit_cast(float, u); }
__device__ __forceinline__ u32 asu(float f) { return __builtin_bit_cast(u32, f); }
__device__ __forceinline__ float bflo(u32 u) { return asf(u << 16); }
__device__ __forceinline__ float bfhi(u32 u) { return asf(u & 0xffff0000u); }
__device__ __forceinline__ u16 f2bf(float f) { return __builtin_bit_cast(u16, (__bf16)f); }
__device__ __forceinline__ float rbf(float f) { return asf((u32)f2bf(f) << 16); }
__device__ __forceinline__ u32 pk2bf(float lo, float hi) { bf16x2 v = {(__bf16)lo, (__bf16)hi}; return __builtin_bit_cast(u32, v); }
template <int CTRL> __device__ __forceinline__ float dpp(float x) {
  return __builtin_bit_cast(float, __builtin_amdgcn_mov_dpp(__builtin_bit_cast(int, x), CTRL, 0xf, 0xf, true));
}
__device__ __forceinline__ float x16_sum(float x) { auto s = __builtin_amdgcn_permlane16_swap(asu(x), asu(x), false, false); return asf(s[0]) + asf(s[1]); }
__device__ __forceinline__ float x32_sum(float x) { auto s = __builtin_amdgcn_permlane32_swap(asu(x), asu(x), false, false); return asf(s[0]) + asf(s[1]); }
__device__ __forceinline__ float x16_max(float x) { auto s = __builtin_amdgcn_permlane16_swap(asu(x), asu(x), false, false); return fmaxf(asf(s[0]), asf(s[1])); }
__device__ __forceinline__ float row_sum(float x) { x += dpp<0xB1>(x); x += dpp<0x4E>(x); x += dpp<0x141>(x); x += dpp<0x128>(x); return x; }
__device__ __forceinline__ float row_max(float x) {
  x = fmaxf(x, dpp<0xB1>(x)); x = fmaxf(x, dpp<0x4E>(x)); x = fmaxf(x, dpp<0x141>(x)); x = fmaxf(x, dpp<0x128>(x)); return x;
}
__device__ __forceinline__ float wave_sum(float x) { return x32_sum(x16_sum(row_sum(x))); }
__device__ __forceinline__ f32x4 mfma16(u32x4 a, u32x4 b, f32x4 c) {
  return __builtin_amdgcn_mfma_f32_16x16x32_bf16(__builtin_bit_cast(bf16x8, a), __builtin_bit_cast(bf16x8, b), c, 0, 0, 0);
}
template <int N> __device__ __forceinline__ void waitvm() { asm volatile("s_waitcnt vmcnt(%0)" ::"i"(N) : "memory"); }
__device__ __forceinline__ void waitlgkm() { asm volatile("s_waitcnt lgkmcnt(0)" ::: "memory"); }
__device__ __forceinline__ void wg_barrier() { asm volatile("s_waitcnt lgkmcnt(0)\n\ts_barrier" ::: "memory"); }
#define POLKV " nt"
#define POLV POLKV
#define POLF " nt"
#define POLW " nt"
__device__ __forceinline__ void glds16(const void* sbase, u32 voff, u32 lds_dst) {
  u32 keep;
  asm volatile("s_mov_b32 %0, m0\n\ts_mov_b32 m0, %3\n\ts_nop 0\n\tglobal_load_lds_dwordx4 %1, %2" POLW "\n\ts_mov_b32 m0, %0"
               : "=&s"(keep) : "v"(voff), "s"(sbase), "s"(lds_dst) : "memory");
}
__device__ __forceinline__ void glds16k(const void* sbase, u32 voff, u32 lds_dst) {
  u32 keep;
  asm volatile("s_mov_b32 %0, m0\n\ts_mov_b32 m0, %3\n\ts_nop 0\n\tglobal_load_lds_dwordx4 %1, %2" POLKV "\n\ts_mov_b32 m0, %0"
               : "=&s"(keep) : "v"(voff), "s"(sbase), "s"(lds_dst) : "memory");
}
__device__ __forceinline__ u32x4 tr_frag(u32 a0, u32 a1) {
  u32x2 r0, r1;
  asm volatile("ds_read_b64_tr_b16 %0, %2\n\tds_read_b64_tr_b16 %1, %3\n\ts_waitcnt lgkmcnt(0)" : "=&v"(r0), "=&v"(r1) : "v"(a0), "v"(a1) : "memory");
  return u32x4{r0.x, r0.y, r1.x, r1.y};
}

struct Tag { u32 hi, lo; };
__device__ __forceinline__ void put(gu64* p, float v, Tag t) {
  __hip_atomic_store(p, ((u64)t.hi << 32) | (u64)((asu(v) & 0xffffff00u) | t.lo), RLX_AGENT);
}
__device__ __forceinline__ u32 tag_bad(u64 g, Tag t) { return (((u32)(g >> 32)) ^ t.hi) | (((u32)g ^ t.lo) & 0xffu); }
__device__ __forceinline__ float dec(u64 g) { return asf((u32)g & 0xffffff00u); }
template <int N> __device__ __forceinline__ void sweep(const gu64* p, int S, Tag t, float (&v)[N]) {
  for (int spins = 0; spins < SPIN_CAP; spins++) {
    u64 g[N];
    u32 bh = 0, bl = 0;
#pragma unroll
    for (int k = 0; k < N; k++) g[k] = __hip_atomic_load(p + k * S, RLX_AGENT);
#pragma unroll
    for (int k = 0; k < N; k++) { bh |= (u32)(g[k] >> 32) ^ t.hi; bl |= (u32)g[k] ^ t.lo; v[k] = dec(g[k]); }
    if (__all((bh | (bl & 0xffu)) == 0u)) break;
  }
}
__device__ __forceinline__ void slow_start(const gu64* p, Tag t) {
  for (int spins = 0; spins < SPIN_CAP; spins++) {
    if (__any(tag_bad(__hip_atomic_load(p, RLX_AGENT), t) == 0u)) break;
    __builtin_amdgcn_s_sleep(SLOWS);
  }
}

__device__ __forceinline__ void cnt_add(u32 addr) {
  u64 keep;
  asm volatile("s_waitcnt lgkmcnt(0)\n\ts_mov_b64 %0, exec\n\ts_mov_b64 exec, 1\n\tds_add_u32 %1, %2\n\ts_mov_b64 exec, %0"
               : "=&s"(keep) : "v"(addr), "v"(1u) : "memory");
}
__device__ __forceinline__ void cnt_wait(LAS u32* c, u32 n) {
  while (__hip_atomic_load(c, RLX_WG) < n) __builtin_amdgcn_s_sleep(1);
  asm volatile("" ::: "memory");
}
__device__ __forceinline__ void cnt_spin(LAS u32* c, u32 n) {
  while (__hip_atomic_load(c, RLX_WG) < n) {}
  asm volatile("" ::: "memory");
}

#define STAMP(i) do {} while (0)
#define FLUSH() do {} while (0)

__global__ void __launch_bounds__(NT, 4) fused_block_kernel(Params P) {
  __shared__ __attribute__((aligned(16))) unsigned char smem[L_END];
  LAS unsigned char* L = (LAS unsigned char*)smem;
  const u32 lds0 = (u32)(size_t)smem;
  const u64 t_start = __builtin_amdgcn_s_memrealtime();
  const int wg = blockIdx.x;
  const int wave = __builtin_amdgcn_readfirstlane(threadIdx.x >> 6), l = threadIdx.x & 63;
  const int n = l & 15, g = l >> 4;
  const int xcd = wg & 7, idx = wg >> 3;
  const int kvh = (wg >> 4) & 7, b = wg >> 7, s = wg & 15;
  const int pair = b * 8 + kvh, aidx = b * 16 + s;
  gu64* ws = (gu64*)P.ws + G_BASE;
  LAS u32* cnt = (LAS u32*)(L + L_CNT);
  const u32 cnt0 = lds0 + L_CNT;

  if (wave < 8) {
    const int w = wave, hf = l >> 5, c = l & 31;
    if (threadIdx.x < 16) cnt[threadIdx.x] = 0u;
    STAMP(0);
    const unsigned char* kb = (const unsigned char*)P.kc + ((size_t)pair * 4096 + s * 256) * 512;
    const unsigned char* vb = (const unsigned char*)P.vc + ((size_t)pair * 4096 + s * 256) * 512;
    const int wr = (w + b) & 7;
    const u32 koff = hf * 512 + ((c ^ ((2 * wr + hf) & 15)) * 16);
    wg_barrier();
#pragma unroll
    for (int i = 0; i < 16; i++) {
      const int gc = ((i + s) & 15) * 8 + wr;
      glds16k(kb + gc * 1024, koff, lds0 + L_K + gc * 1024);
      if (i >= WIN - 1) waitvm<WIN - 1>();
      if (i == QTRIG + WIN - 2) cnt_add(cnt0 + 4 * C_KT);
    }
    u32x4 v[16];
    const u32 voff = g * 512 + n * 16;
    bool open = false;
#pragma unroll
    for (int i = 0; i < 16; i++) {
      const int gc2 = (((i >> 1) + s) & 7) * 8 + wr;
      asm volatile("global_load_dwordx4 %0, %1, %2 offset:%3" POLV : "=v"(v[i]) : "v"(voff), "s"(vb + gc2 * 2048), "i"((i & 1) * 256) : "memory");
      if (i < 15 && !open) waitvm<WIN - 1>();
      if (i == WIN - 2) cnt_add(cnt0 + 4 * C_K);
      if (OPENW && i >= WIN - 2 && i < 15 && !open) {
        const u64 qk = __hip_atomic_load((LAS u64*)(cnt + C_Q), RLX_WG);
        open = __builtin_amdgcn_readfirstlane((u32)qk + (u32)(qk >> 32)) >= 16;
      }
    }
    cnt_add(cnt0 + 4 * C_VISS);
    STAMP(1);
    cnt_wait(cnt + C_P, 8);
    STAMP(18);
    const u32x4 pf = *(LAS u32x4*)(L + L_P + (n >> 3) * 512 + wr * 64 + g * 16);
    const u32x4 zero4 = {0, 0, 0, 0};
    const u32x4 pa0 = (n & 4) ? zero4 : pf, pa1 = (n & 4) ? pf : zero4;
    const Tag tg{*(LAS u32*)(L + L_TAG), *(LAS u32*)(L + L_TAG + 4)};
    gu64* dst = ws + G_P1 + ((kvh * 16 + 2 * w + hf) * 2 + b) * 512 + s * 32 + c;
    asm volatile("" : "+v"(dst));
    u32x4 bf0[8], bf1[8];
#pragma unroll
    for (int t = 0; t < 4; t++) {
      asm volatile("s_waitcnt vmcnt(%4)" : "+v"(v[4 * t]), "+v"(v[4 * t + 1]), "+v"(v[4 * t + 2]), "+v"(v[4 * t + 3]) : "i"(12 - 4 * t) : "memory");
#pragma unroll
      for (int e = 0; e < 8; e++) {
        const u32 sel = (e & 1) ? 0x07060302u : 0x05040100u;
        bf0[e][t] = __builtin_amdgcn_perm(v[4 * t + 2][e >> 1], v[4 * t][e >> 1], sel);
        bf1[e][t] = __builtin_amdgcn_perm(v[4 * t + 3][e >> 1], v[4 * t + 1][e >> 1], sel);
      }
    }
    f32x4 d[8];
#pragma unroll
    for (int e = 0; e < 8; e++) d[e] = mfma16(pa0, bf0[e], f32x4{0.f, 0.f, 0.f, 0.f});
#pragma unroll
    for (int e = 0; e < 8; e++) d[e] = mfma16(pa1, bf1[e], d[e]);
    *(LAS f32x4*)(L + L_RED + w * 2048 + l * 32) = f32x4{d[0][0], d[1][0], d[2][0], d[3][0]};
    *(LAS f32x4*)(L + L_RED + w * 2048 + l * 32 + 16) = f32x4{d[4][0], d[5][0], d[6][0], d[7][0]};
    cnt_add(cnt0 + 4 * C_PV);
    STAMP(19);
    float o = 0.f;
    for (int spins = 0; spins < (1 << 22); spins++) {
      u32 x[8];
      asm volatile("" ::: "memory");
#pragma unroll
      for (int ww = 0; ww < 8; ww++) x[ww] = *(volatile LAS u32*)(L + L_RED + ww * 2048 + (64 * w + l) * 4);
      const u32 mx = max(max(max(x[0], x[1]), max(x[2], x[3])), max(max(x[4], x[5]), max(x[6], x[7])));
      o = ((asf(x[0]) + asf(x[1])) + (asf(x[2]) + asf(x[3]))) + ((asf(x[4]) + asf(x[5])) + (asf(x[6]) + asf(x[7])));
      if (__all(mx != 0xffffffffu)) break;
    }
    put(dst, o, tg);
    cnt_add(cnt0 + 4 * C_DONE);
    STAMP(3);
    FLUSH();
    return;
  }

  const int h = wave - 8, q4 = n >> 2, p4 = n & 3;
  Tag tg;
  {
    const int cg = 8 * kvh + (aidx >> 2), kq = aidx & 3;
    const unsigned char* wqs = (const unsigned char*)P.wq + (size_t)(112 * kq) * 8192 + 128 * cg;
    if (h < 7) {
#pragma unroll
      for (int d = 0; d < 2; d++) glds16(wqs + (size_t)(16 * h + 8 * d) * 8192, (l >> 3) * 8192 + (l & 7) * 16, lds0 + L_WQ + (2 * h + d) * 1024);
    }
    u32x4 xr = {0, 0, 0, 0};
    if (h == 5 || h == 6) xr = *(const u32x4*)(P.x + (h & 1) * 448 + (l < 56 ? l : 55) * 8);
    if (h == 7) {
      u64 ep;
      u32 junk;
      const gu64* epp = ws + G_EPOCH;
      const unsigned char* tp = (const unsigned char*)P.out;
      const int rr = xcd * 16 + idx, fs = xcd * 8 + (idx - 16);
      if (l == 1) tp = (const unsigned char*)P.vc + ((size_t)pair * 4096 + s * 256) * 512;
      if (l == 2) tp = (const unsigned char*)P.vc + ((size_t)pair * 4096 + s * 256 + 255) * 512;
      if (l == 3) tp = (const unsigned char*)P.kc + ((size_t)pair * 4096 + s * 256 + 255) * 512;
      if (l == 4) tp = (const unsigned char*)(ws + G_P1 + kvh * 16384);
      if (l == 5) tp = (const unsigned char*)(ws + G_P1 + 131071);
      if (l == 6) tp = (const unsigned char*)(ws + G_END - 1);
      if (idx < 16) {
        if (l == 7) tp = (const unsigned char*)P.wo + (size_t)rr * 32 * 896;
        if (l == 8) tp = (const unsigned char*)P.wo + (size_t)rr * 32 * 896 + 28668;
      } else if (idx < 24 && xcd < 7) {
        if (l == 7) tp = (const unsigned char*)P.w1 + fs * 64;
        if (l == 8) tp = (const unsigned char*)P.w1 + (size_t)447 * 3584 + fs * 64;
        if (l == 9) tp = (const unsigned char*)P.w1 + (size_t)224 * 3584 + fs * 64;
        if (l == 10) tp = (const unsigned char*)P.w2 + (size_t)fs * 32 * 896;
        if (l == 11) tp = (const unsigned char*)P.w2 + (size_t)fs * 32 * 896 + 28668;
      }
      asm volatile("global_load_dwordx2 %0, %1, off sc1" : "=v"(ep) : "v"(epp) : "memory");
      wg_barrier();
      asm volatile("s_waitcnt vmcnt(0)" : "+v"(ep) :: "memory");
      const u64 ne = ep * 6364136223846793005ull + 1442695040888963407ull;
      if (l == 0) __hip_atomic_store(ws + G_END + 64 + wg, ((u64)(u32)(ne >> 8) << 32) | (u32)t_start, RLX_AGENT);
      if (l == 0) *(LAS u32x4*)(L + L_TAG) = u32x4{(u32)(ne >> 8), (u32)ne & 0xffu, (u32)ne, (u32)(ne >> 32)};
      cnt_add(cnt0 + 4 * C_WQ);
    } else {
      wg_barrier();
      if (h >= 5) {
        float xv[8];
#pragma unroll
        for (int k = 0; k < 4; k++) { xv[2 * k] = bflo(xr[k]); xv[2 * k + 1] = bfhi(xr[k]); }
        float sm = ((xv[0] + xv[1]) + (xv[2] + xv[3])) + ((xv[4] + xv[5]) + (xv[6] + xv[7]));
        const float mean = wave_sum(l < 56 ? sm : 0.f) * (1.f / 448.f);
        float ss = 0.f;
#pragma unroll
        for (int k = 0; k < 8; k++) { const float d = xv[k] - mean; ss = fmaf(d, d, ss); }
        const float var = rbf(wave_sum(l < 56 ? ss : 0.f) * (1.f / 448.f)), mb = rbf(mean);
        const float rden = __builtin_amdgcn_rcpf(rbf(__builtin_amdgcn_sqrtf(rbf(var + 1e-5f))));
        u32x4 o;
#pragma unroll
        for (int k = 0; k < 4; k++) o[k] = pk2bf(rbf(xv[2 * k] - mb) * rden, rbf(xv[2 * k + 1] - mb) * rden);
        const int rl = l - 14 * kq;
        if (rl >= 0 && rl < 14) *(LAS u32x4*)(L + L_XN + (h & 1) * 256 + rl * 16) = o;
        if (l >= 62) *(LAS u32x4*)(L + L_XN + (h & 1) * 256 + (l - 48) * 16) = u32x4{0, 0, 0, 0};
      }
      waitvm<0>();
      cnt_add(cnt0 + 4 * C_WQ);
    }
    cnt_spin(cnt + C_WQ, 8);
    tg.hi = __builtin_amdgcn_readfirstlane(*(LAS u32*)(L + L_TAG));
    tg.lo = __builtin_amdgcn_readfirstlane(*(LAS u32*)(L + L_TAG + 4));
    if (h < 4) {
      f32x4 acc = {0.f, 0.f, 0.f, 0.f};
#pragma unroll
      for (int ks = 0; ks < 4; ks++) {
        const u32x4 af = *(LAS u32x4*)(L + L_XN + (n & 1) * 256 + ks * 64 + g * 16);
        int r0 = 32 * ks + 8 * g + q4;
        if (ks == 3) r0 = (g >= 2) ? r0 - 16 : r0;
        const u32 a = lds0 + L_WQ + r0 * 128 + 32 * h + 8 * p4;
        acc = mfma16(af, tr_frag(a, a + 4 * 128), acc);
      }
      if (g < 2) put(ws + G_QP + (g * 4 + kq) * 4096 + 64 * cg + 16 * h + n, g ? acc[1] : acc[0], tg);
      STAMP(4);
    }
    cnt_wait(cnt + C_KT, 8);
    float qg[4];
    sweep<4>(ws + G_QP + b * 4 * 4096 + kvh * 512 + 64 * h + l, 4096, tg, qg);
    *(LAS u16*)(L + L_Q + (64 * h + l) * 2) = f2bf((qg[0] + qg[1]) + (qg[2] + qg[3]));
    cnt_add(cnt0 + 4 * C_Q);
    STAMP(5);
  }
  {
    cnt_wait(cnt + C_Q, 8);
    u32x4 qf[8];
#pragma unroll
    for (int j = 0; j < 8; j++) qf[j] = *(LAS u32x4*)(L + L_Q + (n & 1) * 512 + j * 64 + g * 16);
    cnt_wait(cnt + C_K, 8);
    STAMP(6);
    const LAS unsigned char* kr = L + L_K + (32 * h + n) * 512;
    u32x4 kf[16];
#pragma unroll
    for (int j = 0; j < 8; j++) {
      kf[2 * j] = *(const LAS u32x4*)(kr + (((4 * j + g) ^ n) * 16));
      kf[2 * j + 1] = *(const LAS u32x4*)(kr + 8192 + (((4 * j + g) ^ n) * 16));
    }
    __builtin_amdgcn_sched_barrier(0);
    f32x4 acc0 = {0.f, 0.f, 0.f, 0.f}, acc1 = {0.f, 0.f, 0.f, 0.f};
#pragma unroll
    for (int j = 0; j < 8; j++) { acc0 = mfma16(qf[j], kf[2 * j], acc0); acc1 = mfma16(qf[j], kf[2 * j + 1], acc1); }
    const float sr = (g & 1) ? ((g >> 1) ? acc1[1] : acc1[0]) : ((g >> 1) ? acc0[1] : acc0[0]);
    const float sc = rbf(sr) * 0.0625f;
    const float mw = x16_max(row_max(sc));
    LAS float* sm = (LAS float*)(L + L_SM);
    if ((l & 31) == 0) sm[(g >> 1) * 8 + h] = mw;
    cnt_add(cnt0 + 4 * C_MAX);
    cnt_spin(cnt + C_MAX, 8);
    const f32x4 ma = *(LAS f32x4*)(sm + (g >> 1) * 8), mb = *(LAS f32x4*)(sm + (g >> 1) * 8 + 4);
    *(LAS u32x4*)(L + L_RED + h * 2048 + l * 32) = u32x4{~0u, ~0u, ~0u, ~0u};
    *(LAS u32x4*)(L + L_RED + h * 2048 + l * 32 + 16) = u32x4{~0u, ~0u, ~0u, ~0u};
    const float m = fmaxf(fmaxf(fmaxf(ma[0], ma[1]), fmaxf(ma[2], ma[3])), fmaxf(fmaxf(mb[0], mb[1]), fmaxf(mb[2], mb[3])));
    const u16 pb = f2bf(__builtin_amdgcn_exp2f((sc - m) * 1.4426950408889634f));
    const int row = 16 * (g & 1) + n;
    *(LAS u16*)(L + L_P + (g >> 1) * 512 + (row >> 2) * 64 + (row & 3) * 16 + ((h - s) & 7) * 2) = pb;
    const float lw = x16_sum(row_sum(bflo((u32)pb)));
    if ((l & 31) == 0) sm[16 + (g >> 1) * 8 + h] = lw;
    cnt_add(cnt0 + 4 * C_P);
    STAMP(7);
    if (h == 7) {
      cnt_spin(cnt + C_P, 8);
      const f32x4 la = *(LAS f32x4*)(sm + 16 + (g >> 1) * 8), lb = *(LAS f32x4*)(sm + 16 + (g >> 1) * 8 + 4);
      const float ls = ((la[0] + la[1]) + (la[2] + la[3])) + ((lb[0] + lb[1]) + (lb[2] + lb[3]));
      if ((l & 30) == 0) put(ws + G_S1 + (((kvh * 2 + (g >> 1)) * 2 + b) * 16 + s) * 2 + (l & 1), (l & 1) ? ls : m, tg);
    }
  }
  const Tag tgo{tg.hi, tg.lo ^ 2u};
  const u32 tgy = tg.hi ^ 0x5bd1e995u;
  const f32x4 z4 = {0.f, 0.f, 0.f, 0.f};
  constexpr u32 SLO = 0x05040100u, SHI = 0x07060302u;
  if (idx < 16) {
    const int r1 = (idx >> 2) * 8 + xcd, cq = idx & 3, cb = 2 * cq + h;
    const bool mm = h < 2 && cb < 7;
    u32x2 wr[4][8];
    cnt_wait(cnt + C_VISS, WAT);
    if (mm) {
      const unsigned char* wsrc = (const unsigned char*)P.wo + (size_t)(r1 * 128 + 8 * g) * 896 + 128 * cb + 8 * n;
#pragma unroll
      for (int ks = 0; ks < 4; ks++)
#pragma unroll
        for (int j = 0; j < 8; j++)
          asm volatile("global_load_dwordx2 %0, %1, off offset:%2" POLW : "=v"(wr[ks][j]) : "v"(wsrc + ks * 32 * 896 + (j >> 2) * 3584), "i"((j & 3) * 896) : "memory");
    }
    const gu64* pd = ws + G_P1 + (4 * r1 + (h >> 1)) * 1024 + (h & 1) * 512 + 2 * l;
    const gu64* ps = ws + G_S1 + ((r1 >> 1) * 2 + (h & 1)) * 32 + (l & 31);
    u32x4 wf[4][4];
    if (mm) {
#pragma unroll
      for (int ks = 0; ks < 4; ks++) {
        asm volatile("s_waitcnt vmcnt(%8)" : "+v"(wr[ks][0]), "+v"(wr[ks][1]), "+v"(wr[ks][2]), "+v"(wr[ks][3]), "+v"(wr[ks][4]), "+v"(wr[ks][5]),
                     "+v"(wr[ks][6]), "+v"(wr[ks][7]) : "i"(24 - 8 * ks) : "memory");
#pragma unroll
        for (int e = 0; e < 4; e++)
#pragma unroll
          for (int t = 0; t < 4; t++) wf[ks][e][t] = __builtin_amdgcn_perm(wr[ks][2 * t + 1][e >> 1], wr[ks][2 * t][e >> 1], (e & 1) ? SHI : SLO);
#pragma unroll
        for (int e = 0; e < 4; e++) asm volatile("" : "+v"(wf[ks][e]));
      }
    }
    GAS u32x4* dst = (GAS u32x4*)(ws + G_Y + r1 * 512 + (g & 1) * 224 + 32 * cb + 2 * n);
    const LAS u32x4* ap = (const LAS u32x4*)(L + L_OB + (n & 1) * 256 + g * 16);
    asm volatile("" : "+v"(dst), "+v"(ap));
    STAMP(8);
    cnt_wait(cnt + C_DONE, 8);
    STAMP(2);
    u32x4 pv[4];
    u64 sg;
    float wk[4];
    u32 pend = 0x1fu;
    for (int spins = 0; spins < 4 * SPIN_CAP && pend; spins++) {
#pragma unroll
      for (int k = 0; k < 4; k++) if (pend & (1u << k))
        asm volatile("global_load_dwordx4 %0, %1, off offset:%2 sc1" : "=v"(pv[k]) : "v"(pd), "i"(k * 1024) : "memory");
      if (pend & 0x10u) asm volatile("global_load_dwordx2 %0, %1, off sc1" : "=v"(sg) : "v"(ps) : "memory");
      asm volatile("s_waitcnt vmcnt(0)" : "+v"(pv[0]), "+v"(pv[1]), "+v"(pv[2]), "+v"(pv[3]), "+v"(sg) :: "memory");
#pragma unroll
      for (int k = 0; k < 4; k++) if (pend & (1u << k)) {
        if (__all(((pv[k][1] ^ tg.hi) | (pv[k][3] ^ tg.hi) | (((pv[k][0] ^ tg.lo) | (pv[k][2] ^ tg.lo)) & 0xffu)) == 0u)) pend &= ~(1u << k);
      }
      if ((pend & 0x10u) && __all(tag_bad(sg, tg) == 0u)) {
        pend &= 0xfu;
        const float st = dec(sg);
        const float M = x16_max(row_max((l & 1) ? -INFINITY : st));
        const float wgt = __builtin_amdgcn_exp2f((st - M) * 1.4426950408889634f);
        const float lw = dpp<0xB1>(wgt) * st;
        const float wn = wgt * __builtin_amdgcn_rcpf(x16_sum(row_sum((l & 1) ? lw : 0.f)));
#pragma unroll
        for (int k = 0; k < 4; k++) wk[k] = asf(__builtin_amdgcn_ds_bpermute(8 * (4 * k + g), asu(wn)));
      }
    }
    STAMP(9);
    {
      float o0 = 0.f, o1 = 0.f;
#pragma unroll
      for (int k = 0; k < 4; k++) { o0 = fmaf(wk[k], asf(pv[k][0] & 0xffffff00u), o0); o1 = fmaf(wk[k], asf(pv[k][2] & 0xffffff00u), o1); }
      o0 = x32_sum(x16_sum(o0)); o1 = x32_sum(x16_sum(o1));
      if (l < 16) *(LAS u32*)(L + L_OB + (h & 1) * 256 + (h >> 1) * 64 + n * 4) = pk2bf(o0, o1);
    }
    wg_barrier();
    if (mm) {
      u32x4 af[4];
#pragma unroll
      for (int ks = 0; ks < 4; ks++) af[ks] = ap[4 * ks];
      f32x4 d[4] = {z4, z4, z4, z4};
#pragma unroll
      for (int ks = 0; ks < 4; ks++)
#pragma unroll
        for (int e = 0; e < 4; e++) d[e] = mfma16(af[ks], wf[ks][e], d[e]);
      const u32 lo = (g & 1) ? pk2bf(d[0][1], d[1][1]) : pk2bf(d[0][0], d[1][0]);
      const u32 hi = (g & 1) ? pk2bf(d[2][1], d[3][1]) : pk2bf(d[2][0], d[3][0]);
      const u32x4 val = {lo, tgy, hi, tgy};
      if (YREP == 1) {
        if (g < 2) asm volatile("global_store_dwordx4 %0, %1, off sc1\n\ts_nop 1" :: "v"(dst), "v"(val) : "memory");
      } else {
#pragma unroll
        for (int c2 = 0; c2 < (YREP + 1) / 2; c2++)
          if (2 * c2 + 1 < YREP || g < 2)
            asm volatile("global_store_dwordx4 %0, %1, off sc1\n\ts_nop 1" :: "v"(dst + (2 * c2 + (g >> 1)) * (32 * 256)), "v"(val) : "memory");
      }
      STAMP(10);
    }
    if (h == 7 || cq < 2 || r1 >= 28) { FLUSH(); return; }
    const int t = 2 * r1 + cq - 2, tb = t >= 28 ? 1 : 0, col = 16 * t - 448 * tb;
    u32 hpk = 0;
    for (int spins = 0; spins < SPIN_CAP; spins++) {
      const u64 gg = __hip_atomic_load(ws + G_H + 8 * t + (n >> 1), RLX_AGENT);
      hpk = (u32)gg;
      if (__all((u32)(gg >> 32) == tgy)) break;
      __builtin_amdgcn_s_sleep(SLOWS);
    }
    STAMP(17);
    float fv[2];
    sweep<2>(ws + G_P1 + (tb * 56 + 8 * h + g) * 1024 + 512 + col + n, 4096, tgo, fv);
    STAMP(11);
    const float y = x32_sum(x16_sum(fv[0] + fv[1]));
    if (l < 16) *(LAS float*)(L + L_UR + h * 64 + n * 4) = y;
    wg_barrier();
    if (h == 0) {
      float yy = 0.f;
#pragma unroll
      for (int k = 0; k < 7; k++) yy += *(LAS float*)(L + L_UR + k * 64 + n * 4);
      if (t == 0) {
        const u32 now = (u32)__builtin_amdgcn_s_memrealtime();
        u32 mx = 0, mn = 0xffffffffu;
        for (int k = 0; k < 4; k++) { const u32 age = now - (u32)__hip_atomic_load(ws + G_END + 64 + k * 64 + l, RLX_AGENT); mx = max(mx, age); mn = min(mn, age); }
        for (int o = 32; o > 0; o >>= 1) { mx = max(mx, (u32)__shfl_xor((int)mx, o)); mn = min(mn, (u32)__shfl_xor((int)mn, o)); }
        const u32 delay = DIAGMODE == 1 ? mx * 4 : DIAGMODE == 2 ? (mx - mn) * 10 : 0;
        const u32 t1 = (u32)__builtin_amdgcn_s_memrealtime();
        while ((u32)__builtin_amdgcn_s_memrealtime() - t1 < min(delay, 100000u)) __builtin_amdgcn_s_sleep(2);
      }
      if (l < 16) P.out[16 * t + n] = f2bf(((n & 1) ? bfhi(hpk) : bflo(hpk)) + rbf(yy));
      if (t == 0 && l == 0) __hip_atomic_store(ws + G_EPOCH, *(LAS u64*)(L + L_TAG + 8), RLX_AGENT);
      STAMP(12);
    }
  } else if (xcd < 7) {
    if (h == 7) { FLUSH(); return; }
    const int fs = xcd * 8 + ((idx - 16) >> 1), fb = idx & 1;
    u32 w1r[2][8];
    u32x2 w2r[8];
    cnt_wait(cnt + C_VISS, WAT);
    if (WDEFER == 2) cnt_wait(cnt + C_DONE, 8);
    if (WDEFER == 3) cnt_wait(cnt + C_PV, FPV);
    {
      const unsigned char* s1 = (const unsigned char*)P.w1 + (size_t)(64 * h + 8 * g) * 3584 + 64 * fs + 4 * n;
      const unsigned char* s2 = (const unsigned char*)P.w2 + (size_t)(32 * fs + 8 * g) * 896 + 128 * h + 8 * n;
#pragma unroll
      for (int a = 0; a < 2; a++)
#pragma unroll
        for (int j = 0; j < 8; j++) asm volatile("global_load_dword %0, %1, off" POLF : "=v"(w1r[a][j]) : "v"(s1 + (32 * a + j) * 3584) : "memory");
#pragma unroll
      for (int j = 0; j < 8; j++)
        asm volatile("global_load_dwordx2 %0, %1, off offset:%2" POLF : "=v"(w2r[j]) : "v"(s2 + (j >> 2) * 3584), "i"((j & 3) * 896) : "memory");
    }
    const u32x2 xw = *(const u32x2*)(P.x + fb * 448 + 64 * h + 4 * n);
    asm volatile("s_waitcnt vmcnt(0)" : "+v"(w1r[0][0]), "+v"(w1r[0][1]), "+v"(w1r[0][2]), "+v"(w1r[0][3]), "+v"(w1r[0][4]), "+v"(w1r[0][5]), "+v"(w1r[0][6]),
                 "+v"(w1r[0][7]), "+v"(w1r[1][0]), "+v"(w1r[1][1]), "+v"(w1r[1][2]), "+v"(w1r[1][3]), "+v"(w1r[1][4]), "+v"(w1r[1][5]), "+v"(w1r[1][6]),
                 "+v"(w1r[1][7]) :: "memory");
    asm volatile("" : "+v"(w2r[0]), "+v"(w2r[1]), "+v"(w2r[2]), "+v"(w2r[3]), "+v"(w2r[4]), "+v"(w2r[5]), "+v"(w2r[6]), "+v"(w2r[7]) :: "memory");
    u32x4 w1f[2][2], w2f[4];
#pragma unroll
    for (int t = 0; t < 4; t++) {
#pragma unroll
      for (int a = 0; a < 2; a++) {
        w1f[a][0][t] = __builtin_amdgcn_perm(w1r[a][2 * t + 1], w1r[a][2 * t], SLO);
        w1f[a][1][t] = __builtin_amdgcn_perm(w1r[a][2 * t + 1], w1r[a][2 * t], SHI);
      }
#pragma unroll
      for (int e = 0; e < 4; e++) w2f[e][t] = __builtin_amdgcn_perm(w2r[2 * t + 1][e >> 1], w2r[2 * t][e >> 1], (e & 1) ? SHI : SLO);
    }
    const u32x4 ones = {0x3f803f80u, 0x3f803f80u, 0x3f803f80u, 0x3f803f80u};
    float cs;
    {
      f32x4 c0 = mfma16(ones, w1f[0][0], z4), c1 = mfma16(ones, w1f[0][1], z4);
      c0 = mfma16(ones, w1f[1][0], c0); c1 = mfma16(ones, w1f[1][1], c1);
      if (g < 2) *(LAS float*)(L + L_UR + h * 160 + (2 * n + g) * 4) = g ? c1[0] : c0[0];
      wg_barrier();
      cs = 0.f;
#pragma unroll
      for (int k = 0; k < 7; k++) cs += *(LAS float*)(L + L_UR + k * 160 + (l & 31) * 4);
      wg_barrier();
    }
    gu64* dst = ws + G_P1 + (fb * 56 + fs) * 1024 + 512 + 64 * h + 4 * n + g;
    const gu64* src = ws + G_Y + (xcd % YREP) * (32 * 512) + g * 512 + fb * 224 + 32 * h + 2 * n;
    asm volatile("" : "+v"(dst), "+v"(src), "+v"(cs));
#pragma unroll
    for (int e = 0; e < 4; e++) asm volatile("" : "+v"(w2f[e]));
    asm volatile("" : "+v"(w1f[0][0]), "+v"(w1f[0][1]), "+v"(w1f[1][0]), "+v"(w1f[1][1]));
    STAMP(13);
    for (int spins = 0; FSLOW && spins < SPIN_CAP; spins++) {
      if (__any((u32)(__hip_atomic_load(src + h * 2048, RLX_AGENT) >> 32) == tgy)) break;
      __builtin_amdgcn_s_sleep(SLOWS);
    }
    u32x4 yv[8];
    {
      u32 pend = 0xffu;
      for (int spins = 0; spins < 4 * SPIN_CAP && pend; spins++) {
#pragma unroll
        for (int k = 0; k < 8; k++) if (pend & (1u << k))
          asm volatile("global_load_dwordx4 %0, %1, off sc1" : "=v"(yv[k]) : "v"(src + k * 2048) : "memory");
        asm volatile("s_waitcnt vmcnt(0)" : "+v"(yv[0]), "+v"(yv[1]), "+v"(yv[2]), "+v"(yv[3]), "+v"(yv[4]), "+v"(yv[5]), "+v"(yv[6]), "+v"(yv[7]) :: "memory");
#pragma unroll
        for (int k = 0; k < 8; k++) if (pend & (1u << k)) {
          if (__all(((yv[k][1] ^ tgy) | (yv[k][3] ^ tgy)) == 0u)) pend &= ~(1u << k);
        }
      }
    }
    STAMP(16);
    u32x2 hp;
    {
      float y[4] = {0.f, 0.f, 0.f, 0.f};
#pragma unroll
      for (int k = 0; k < 8; k++) { y[0] += bflo(yv[k][0]); y[1] += bfhi(yv[k][0]); y[2] += bflo(yv[k][2]); y[3] += bfhi(yv[k][2]); }
#pragma unroll
      for (int e = 0; e < 4; e++) y[e] = x32_sum(x16_sum(y[e]));
      hp.x = pk2bf(bflo(xw.x) + rbf(y[0]), bfhi(xw.x) + rbf(y[1]));
      hp.y = pk2bf(bflo(xw.y) + rbf(y[2]), bfhi(xw.y) + rbf(y[3]));
      if (l < 16) *(LAS u32x2*)(L + L_FB + h * 128 + n * 8) = hp;
    }
    waitlgkm();
    const u32x4 af0 = *(LAS u32x4*)(L + L_FB + h * 128 + g * 16), af1 = *(LAS u32x4*)(L + L_FB + h * 128 + 64 + g * 16);
    if (fs == 0 && l < 32) __hip_atomic_store(ws + G_H + fb * 224 + 32 * h + 2 * n + g, ((u64)tgy << 32) | (g ? hp.y : hp.x), RLX_AGENT);
    STAMP(14);
    f32x4 u0, u1, st;
    {
      const u32x4 b0 = n == 0 ? af0 : ones, b1 = n == 0 ? af1 : ones;
      u0 = mfma16(af0, w1f[0][0], z4); u1 = mfma16(af0, w1f[0][1], z4); st = mfma16(af0, b0, z4);
      u0 = mfma16(af1, w1f[1][0], u0); u1 = mfma16(af1, w1f[1][1], u1); st = mfma16(af1, b1, st);
    }
    {
      const float uv = (g == 0) ? u0[0] : (g == 1) ? u1[0] : st[0];
      if (g < 2 || l < 34) *(LAS float*)(L + L_UR + h * 160 + (g < 2 ? 2 * n + g : 32 + n) * 4) = uv;
    }
    wg_barrier();
    {
      float ua = 0.f;
      f32x2 sa = {0.f, 0.f};
#pragma unroll
      for (int k = 0; k < 7; k++) {
        ua += *(LAS float*)(L + L_UR + k * 160 + (l & 31) * 4);
        sa += *(LAS f32x2*)(L + L_UR + k * 160 + 128);
      }
      const float mean = sa[1] * (1.f / 448.f);
      const float var = rbf(fmaxf(fmaf(-mean, mean, sa[0] * (1.f / 448.f)), 0.f));
      const float rden = __builtin_amdgcn_rcpf(rbf(__builtin_amdgcn_sqrtf(rbf(var + 1e-5f))));
      const float u = rbf(fmaf(-rbf(mean), cs, ua) * rden);
      const float f = u * __builtin_amdgcn_rcpf(1.f + __builtin_amdgcn_exp2f(-u * 1.4426950408889634f));
      if (l < 32) *(LAS u16*)(L + L_FB + h * 128 + l * 2) = f2bf(f);
    }
    waitlgkm();
    const u32x4 ff = *(LAS u32x4*)(L + L_FB + h * 128 + g * 16);
    f32x4 d[4];
#pragma unroll
    for (int e = 0; e < 4; e++) d[e] = mfma16(ff, w2f[e], z4);
    put(dst, (g >> 1) ? ((g & 1) ? d[3][0] : d[2][0]) : ((g & 1) ? d[1][0] : d[0][0]), tgo);
    STAMP(15);
  }
  FLUSH();
}

extern "C" void kernel_launch(void* const* d_in, const int* in_sizes, int n_in, void* d_out, int out_size, void* d_ws, size_t ws_size,
                              hipStream_t stream) {
  Params p;
  p.x = (const u16*)d_in[0]; p.kc = (const u16*)d_in[1]; p.vc = (const u16*)d_in[2]; p.wq = (const u16*)d_in[3]; p.wo = (const u16*)d_in[4];
  p.w1 = (const u16*)d_in[6]; p.w2 = (const u16*)d_in[7]; p.out = (u16*)d_out; p.ws = (u64*)d_ws;
  fused_block_kernel<<<256, NT, 0, stream>>>(p);
}
```

```cpp
#define PRETOUCH 0
#include <hip/hip_runtime.h>
#include <stdint.h>

typedef unsigned short u16;
typedef unsigned u32;
typedef unsigned long long u64;
typedef u32 u32x4 __attribute__((ext_vector_type(4)));
typedef u32 u32x2 __attribute__((ext_vector_type(2)));
typedef float f32x4 __attribute__((ext_vector_type(4)));
typedef float f32x2 __attribute__((ext_vector_type(2)));
typedef __bf16 bf16x8 __attribute__((ext_vector_type(8)));
typedef __bf16 bf16x2 __attribute__((ext_vector_type(2)));
#define GAS __attribute__((address_space(1)))
#define LAS __attribute__((address_space(3)))
typedef GAS u64 gu64;
#define RLX_AGENT __ATOMIC_RELAXED, __HIP_MEMORY_SCOPE_AGENT
#define RLX_WG __ATOMIC_RELAXED, __HIP_MEMORY_SCOPE_WORKGROUP

#define WIN 4
#define WDEFER 2
#define WODEFER 1
#define QTRIG 6
#define OPENW 1
#define XCDMAP 0
#define SLOWS 4
#define NOSTREAM 0
#define FSLOW 0
#define FPV 2
#define YREP 1
#define WOPV 0
#define WODONE 0
#define KFIRST -1
#define WAT 8
#define G_BASE 0
static_assert(QTRIG + WIN - 2 < 16, "trigger inside the K loop");

constexpr int NT = 1024;
constexpr int SPIN_CAP = 1 << 16;
constexpr int G_EPOCH = 0;
constexpr int G_QP = 512;
constexpr int G_P1 = G_QP + 32768;
constexpr int G_S1 = G_P1 + 131072;
constexpr int G_H = G_S1 + 1024;
constexpr int G_Y = G_H + 512;
constexpr int G_END = G_Y + YREP * 32 * 512;
constexpr int G_STAMP = 1 << 20;
constexpr int L_K = 0;
constexpr int L_RED = 65536;
constexpr int L_WQ = 131072;
constexpr int L_XN = L_WQ + 14336;
constexpr int L_Q = L_XN + 512;
constexpr int L_P = L_Q + 1024;
constexpr int L_SM = L_P + 1024;
constexpr int L_CNT = L_SM + 128;
constexpr int L_TAG = L_CNT + 64;
constexpr int L_OB = L_TAG + 16;
constexpr int L_UR = L_OB + 512;
constexpr int L_FB = L_UR + 8 * 320;
constexpr int L_END = L_FB + 1024;
enum { C_Q = 0, C_K, C_WQ, C_KT, C_MAX, C_P, C_VISS, C_DONE, C_W, C_PV, C_N };

struct Params {
  const u16* x; const u16* kc; const u16* vc; const u16* wq; const u16* wo; const u16* w1; const u16* w2; u16* out; u64* ws;
};

__device__ __forceinline__ float asf(u32 u) { return __builtin_bit_cast(float, u); }
__device__ __forceinline__ u32 asu(float f) { return __builtin_bit_cast(u32, f); }
__device__ __forceinline__ float bflo(u32 u) { return asf(u << 16); }
__device__ __forceinline__ float bfhi(u32 u) { return asf(u & 0xffff0000u); }
__device__ __forceinline__ u16 f2bf(float f) { return __builtin_bit_cast(u16, (__bf16)f); }
__device__ __forceinline__ float rbf(float f) { return asf((u32)f2bf(f) << 16); }
__device__ __forceinline__ u32 pk2bf(float lo, float hi) { bf16x2 v = {(__bf16)lo, (__bf16)hi}; return __builtin_bit_cast(u32, v); }
template <int CTRL> __device__ __forceinline__ float dpp(float x) {
  return __builtin_bit_cast(float, __builtin_amdgcn_mov_dpp(__builtin_bit_cast(int, x), CTRL, 0xf, 0xf, true));
}
__device__ __forceinline__ float x16_sum(float x) { auto s = __builtin_amdgcn_permlane16_swap(asu(x), asu(x), false, false); return asf(s[0]) + asf(s[1]); }
__device__ __forceinline__ float x32_sum(float x) { auto s = __builtin_amdgcn_permlane32_swap(asu(x), asu(x), false, false); return asf(s[0]) + asf(s[1]); }
__device__ __forceinline__ float x16_max(float x) { auto s = __builtin_amdgcn_permlane16_swap(asu(x), asu(x), false, false); return fmaxf(asf(s[0]), asf(s[1])); }
__device__ __forceinline__ float row_sum(float x) { x += dpp<0xB1>(x); x += dpp<0x4E>(x); x += dpp<0x141>(x); x += dpp<0x128>(x); return x; }
__device__ __forceinline__ float row_max(float x) {
  x = fmaxf(x, dpp<0xB1>(x)); x = fmaxf(x, dpp<0x4E>(x)); x = fmaxf(x, dpp<0x141>(x)); x = fmaxf(x, dpp<0x128>(x)); return x;
}
__device__ __forceinline__ float wave_sum(float x) { return x32_sum(x16_sum(row_sum(x))); }
__device__ __forceinline__ f32x4 mfma16(u32x4 a, u32x4 b, f32x4 c) {
  return __builtin_amdgcn_mfma_f32_16x16x32_bf16(__builtin_bit_cast(bf16x8, a), __builtin_bit_cast(bf16x8, b), c, 0, 0, 0);
}
template <int N> __device__ __forceinline__ void waitvm() { asm volatile("s_waitcnt vmcnt(%0)" ::"i"(N) : "memory"); }
__device__ __forceinline__ void waitlgkm() { asm volatile("s_waitcnt lgkmcnt(0)" ::: "memory"); }
__device__ __forceinline__ void wg_barrier() { asm volatile("s_waitcnt lgkmcnt(0)\n\ts_barrier" ::: "memory"); }
#define POLKV " nt"
#define POLV POLKV
#define POLF " nt"
#define POLW " nt"
__device__ __forceinline__ void glds16(const void* sbase, u32 voff, u32 lds_dst) {
  u32 keep;
  asm volatile("s_mov_b32 %0, m0\n\ts_mov_b32 m0, %3\n\ts_nop 0\n\tglobal_load_lds_dwordx4 %1, %2" POLW "\n\ts_mov_b32 m0, %0"
               : "=&s"(keep) : "v"(voff), "s"(sbase), "s"(lds_dst) : "memory");
}
__device__ __forceinline__ void glds16k(const void* sbase, u32 voff, u32 lds_dst) {
  u32 keep;
  asm volatile("s_mov_b32 %0, m0\n\ts_mov_b32 m0, %3\n\ts_nop 0\n\tglobal_load_lds_dwordx4 %1, %2" POLKV "\n\ts_mov_b32 m0, %0"
               : "=&s"(keep) : "v"(voff), "s"(sbase), "s"(lds_dst) : "memory");
}
__device__ __forceinline__ u32x4 tr_frag(u32 a0, u32 a1) {
  u32x2 r0, r1;
  asm volatile("ds_read_b64_tr_b16 %0, %2\n\tds_read_b64_tr_b16 %1, %3\n\ts_waitcnt lgkmcnt(0)" : "=&v"(r0), "=&v"(r1) : "v"(a0), "v"(a1) : "memory");
  return u32x4{r0.x, r0.y, r1.x, r1.y};
}

struct Tag { u32 hi, lo; };
__device__ __forceinline__ void put(gu64* p, float v, Tag t) {
  __hip_atomic_store(p, ((u64)t.hi << 32) | (u64)((asu(v) & 0xffffff00u) | t.lo), RLX_AGENT);
}
__device__ __forceinline__ u32 tag_bad(u64 g, Tag t) { return (((u32)(g >> 32)) ^ t.hi) | (((u32)g ^ t.lo) & 0xffu); }
__device__ __forceinline__ float dec(u64 g) { return asf((u32)g & 0xffffff00u); }
template <int N> __device__ __forceinline__ void sweep(const gu64* p, int S, Tag t, float (&v)[N]) {
  for (int spins = 0; spins < SPIN_CAP; spins++) {
    u64 g[N];
    u32 bh = 0, bl = 0;
#pragma unroll
    for (int k = 0; k < N; k++) g[k] = __hip_atomic_load(p + k * S, RLX_AGENT);
#pragma unroll
    for (int k = 0; k < N; k++) { bh |= (u32)(g[k] >> 32) ^ t.hi; bl |= (u32)g[k] ^ t.lo; v[k] = dec(g[k]); }
    if (__all((bh | (bl & 0xffu)) == 0u)) break;
  }
}
__device__ __forceinline__ void slow_start(const gu64* p, Tag t) {
  for (int spins = 0; spins < SPIN_CAP; spins++) {
    if (__any(tag_bad(__hip_atomic_load(p, RLX_AGENT), t) == 0u)) break;
    __builtin_amdgcn_s_sleep(SLOWS);
  }
}

__device__ __forceinline__ void cnt_add(u32 addr) {
  u64 keep;
  asm volatile("s_waitcnt lgkmcnt(0)\n\ts_mov_b64 %0, exec\n\ts_mov_b64 exec, 1\n\tds_add_u32 %1, %2\n\ts_mov_b64 exec, %0"
               : "=&s"(keep) : "v"(addr), "v"(1u) : "memory");
}
__device__ __forceinline__ void cnt_wait(LAS u32* c, u32 n) {
  while (__hip_atomic_load(c, RLX_WG) < n) __builtin_amdgcn_s_sleep(1);
  asm volatile("" ::: "memory");
}
__device__ __forceinline__ void cnt_spin(LAS u32* c, u32 n) {
  while (__hip_atomic_load(c, RLX_WG) < n) {}
  asm volatile("" ::: "memory");
}

#define STAMP(i) do {} while (0)
#define FLUSH() do {} while (0)

__global__ void __launch_bounds__(NT, 4) fused_block_kernel(Params P) {
  __shared__ __attribute__((aligned(16))) unsigned char smem[L_END];
  LAS unsigned char* L = (LAS unsigned char*)smem;
  const u32 lds0 = (u32)(size_t)smem;
  const int wg = blockIdx.x;
  const int wave = __builtin_amdgcn_readfirstlane(threadIdx.x >> 6), l = threadIdx.x & 63;
  const int n = l & 15, g = l >> 4;
  const int xcd = wg & 7, idx = wg >> 3;
  const int kvh = (wg >> 4) & 7, b = wg >> 7, s = wg & 15;
  const int pair = b * 8 + kvh, aidx = b * 16 + s;
  gu64* ws = (gu64*)P.ws + G_BASE;
  LAS u32* cnt = (LAS u32*)(L + L_CNT);
  const u32 cnt0 = lds0 + L_CNT;

  if (wave < 8) {
    const int w = wave, hf = l >> 5, c = l & 31;
    if (threadIdx.x < 16) cnt[threadIdx.x] = 0u;
    STAMP(0);
    const unsigned char* kb = (const unsigned char*)P.kc + ((size_t)pair * 4096 + s * 256) * 512;
    const unsigned char* vb = (const unsigned char*)P.vc + ((size_t)pair * 4096 + s * 256) * 512;
    const int wr = (w + b) & 7;
    const u32 koff = hf * 512 + ((c ^ ((2 * wr + hf) & 15)) * 16);
    if (KFIRST < 0) wg_barrier();
#pragma unroll
    for (int i = 0; i < 16; i++) {
      const int gc = ((i + s) & 15) * 8 + wr;
      glds16k(kb + gc * 1024, koff, lds0 + L_K + gc * 1024);
      if (i == KFIRST) wg_barrier();
      if (i >= WIN - 1) waitvm<WIN - 1>();
      if (i == QTRIG + WIN - 2) cnt_add(cnt0 + 4 * C_KT);
    }
    u32x4 v[16];
    const u32 voff = g * 512 + n * 16;
    bool open = false;
#pragma unroll
    for (int i = 0; i < 16; i++) {
      const int gc2 = (((i >> 1) + s) & 7) * 8 + wr;
      asm volatile("global_load_dwordx4 %0, %1, %2 offset:%3" POLV : "=v"(v[i]) : "v"(voff), "s"(vb + gc2 * 2048), "i"((i & 1) * 256) : "memory");
      if (i < 15 && !open) waitvm<WIN - 1>();
      if (i == WIN - 2) cnt_add(cnt0 + 4 * C_K);
      if (OPENW && i >= WIN - 2 && i < 15 && !open) {
        const u64 qk = __hip_atomic_load((LAS u64*)(cnt + C_Q), RLX_WG);
        open = __builtin_amdgcn_readfirstlane((u32)qk + (u32)(qk >> 32)) >= 16;
      }
    }
    cnt_add(cnt0 + 4 * C_VISS);
    STAMP(1);
    cnt_wait(cnt + C_P, 8);
    STAMP(18);
    const u32x4 pf = *(LAS u32x4*)(L + L_P + (n >> 3) * 512 + wr * 64 + g * 16);
    const u32x4 zero4 = {0, 0, 0, 0};
    const u32x4 pa0 = (n & 4) ? zero4 : pf, pa1 = (n & 4) ? pf : zero4;
    const Tag tg{*(LAS u32*)(L + L_TAG), *(LAS u32*)(L + L_TAG + 4)};
    gu64* dst = ws + G_P1 + ((kvh * 16 + 2 * w + hf) * 2 + b) * 512 + s * 32 + c;
    asm volatile("" : "+v"(dst));
    u32x4 bf0[8], bf1[8];
#pragma unroll
    for (int t = 0; t < 4; t++) {
      asm volatile("s_waitcnt vmcnt(%4)" : "+v"(v[4 * t]), "+v"(v[4 * t + 1]), "+v"(v[4 * t + 2]), "+v"(v[4 * t + 3]) : "i"(12 - 4 * t) : "memory");
#pragma unroll
      for (int e = 0; e < 8; e++) {
        const u32 sel = (e & 1) ? 0x07060302u : 0x05040100u;
        bf0[e][t] = __builtin_amdgcn_perm(v[4 * t + 2][e >> 1], v[4 * t][e >> 1], sel);
        bf1[e][t] = __builtin_amdgcn_perm(v[4 * t + 3][e >> 1], v[4 * t + 1][e >> 1], sel);
      }
    }
    f32x4 d[8];
#pragma unroll
    for (int e = 0; e < 8; e++) d[e] = mfma16(pa0, bf0[e], f32x4{0.f, 0.f, 0.f, 0.f});
#pragma unroll
    for (int e = 0; e < 8; e++) d[e] = mfma16(pa1, bf1[e], d[e]);
    *(LAS f32x4*)(L + L_RED + w * 2048 + l * 32) = f32x4{d[0][0], d[1][0], d[2][0], d[3][0]};
    *(LAS f32x4*)(L + L_RED + w * 2048 + l * 32 + 16) = f32x4{d[4][0], d[5][0], d[6][0], d[7][0]};
    cnt_add(cnt0 + 4 * C_PV);
    STAMP(19);
    float o = 0.f;
    for (int spins = 0; spins < (1 << 22); spins++) {
      u32 x[8];
      asm volatile("" ::: "memory");
#pragma unroll
      for (int ww = 0; ww < 8; ww++) x[ww] = *(volatile LAS u32*)(L + L_RED + ww * 2048 + (64 * w + l) * 4);
      const u32 mx = max(max(max(x[0], x[1]), max(x[2], x[3])), max(max(x[4], x[5]), max(x[6], x[7])));
      o = ((asf(x[0]) + asf(x[1])) + (asf(x[2]) + asf(x[3]))) + ((asf(x[4]) + asf(x[5])) + (asf(x[6]) + asf(x[7])));
      if (__all(mx != 0xffffffffu)) break;
    }
    put(dst, o, tg);
    cnt_add(cnt0 + 4 * C_DONE);
    STAMP(3);
    FLUSH();
    return;
  }

  const int h = wave - 8, q4 = n >> 2, p4 = n & 3;
  Tag tg;
  {
    const int cg = 8 * kvh + (aidx >> 2), kq = aidx & 3;
    const unsigned char* wqs = (const unsigned char*)P.wq + (size_t)(112 * kq) * 8192 + 128 * cg;
    if (h < 7) {
#pragma unroll
      for (int d = 0; d < 2; d++) glds16(wqs + (size_t)(16 * h + 8 * d) * 8192, (l >> 3) * 8192 + (l & 7) * 16, lds0 + L_WQ + (2 * h + d) * 1024);
    }
    u32x4 xr = {0, 0, 0, 0};
    if (h == 5 || h == 6) xr = *(const u32x4*)(P.x + (h & 1) * 448 + (l < 56 ? l : 55) * 8);
    if (h == 7) {
      u64 ep;
      u32 junk;
      const gu64* epp = ws + G_EPOCH;
      const unsigned char* tp = (const unsigned char*)P.out;
      const int rr = xcd * 16 + idx, fs = xcd * 8 + (idx - 16);
      if (l == 1) tp = (const unsigned char*)P.vc + ((size_t)pair * 4096 + s * 256) * 512;
      if (l == 2) tp = (const unsigned char*)P.vc + ((size_t)pair * 4096 + s * 256 + 255) * 512;
      if (l == 3) tp = (const unsigned char*)P.kc + ((size_t)pair * 4096 + s * 256 + 255) * 512;
      if (l == 4) tp = (const unsigned char*)(ws + G_P1 + kvh * 16384);
      if (l == 5) tp = (const unsigned char*)(ws + G_P1 + 131071);
      if (l == 6) tp = (const unsigned char*)(ws + G_END - 1);
      if (idx < 16) {
        if (l == 7) tp = (const unsigned char*)P.wo + (size_t)rr * 32 * 896;
        if (l == 8) tp = (const unsigned char*)P.wo + (size_t)rr * 32 * 896 + 28668;
      } else if (idx < 24 && xcd < 7) {
        if (l == 7) tp = (const unsigned char*)P.w1 + fs * 64;
        if (l == 8) tp = (const unsigned char*)P.w1 + (size_t)447 * 3584 + fs * 64;
        if (l == 9) tp = (const unsigned char*)P.w1 + (size_t)224 * 3584 + fs * 64;
        if (l == 10) tp = (const unsigned char*)P.w2 + (size_t)fs * 32 * 896;
        if (l == 11) tp = (const unsigned char*)P.w2 + (size_t)fs * 32 * 896 + 28668;
      }
      asm volatile("global_load_dwordx2 %0, %1, off sc1" : "=v"(ep) : "v"(epp) : "memory");
      wg_barrier();
      asm volatile("s_waitcnt vmcnt(0)" : "+v"(ep) :: "memory");
      const u64 ne = ep * 6364136223846793005ull + 1442695040888963407ull;
      if (l == 0) *(LAS u32x4*)(L + L_TAG) = u32x4{(u32)(ne >> 8), (u32)ne & 0xffu, (u32)ne, (u32)(ne >> 32)};
      cnt_add(cnt0 + 4 * C_WQ);
    } else {
      wg_barrier();
      if (h >= 5) {
        float xv[8];
#pragma unroll
        for (int k = 0; k < 4; k++) { xv[2 * k] = bflo(xr[k]); xv[2 * k + 1] = bfhi(xr[k]); }
        float sm = ((xv[0] + xv[1]) + (xv[2] + xv[3])) + ((xv[4] + xv[5]) + (xv[6] + xv[7]));
        const float mean = wave_sum(l < 56 ? sm : 0.f) * (1.f / 448.f);
        float ss = 0.f;
#pragma unroll
        for (int k = 0; k < 8; k++) { const float d = xv[k] - mean; ss = fmaf(d, d, ss); }
        const float var = rbf(wave_sum(l < 56 ? ss : 0.f) * (1.f / 448.f)), mb = rbf(mean);
        const float rden = __builtin_amdgcn_rcpf(rbf(__builtin_amdgcn_sqrtf(rbf(var + 1e-5f))));
        u32x4 o;
#pragma unroll
        for (int k = 0; k < 4; k++) o[k] = pk2bf(rbf(xv[2 * k] - mb) * rden, rbf(xv[2 * k + 1] - mb) * rden);
        const int rl = l - 14 * kq;
        if (rl >= 0 && rl < 14) *(LAS u32x4*)(L + L_XN + (h & 1) * 256 + rl * 16) = o;
        if (l >= 62) *(LAS u32x4*)(L + L_XN + (h & 1) * 256 + (l - 48) * 16) = u32x4{0, 0, 0, 0};
      }
      waitvm<0>();
      cnt_add(cnt0 + 4 * C_WQ);
    }
    cnt_spin(cnt + C_WQ, 8);
    tg.hi = __builtin_amdgcn_readfirstlane(*(LAS u32*)(L + L_TAG));
    tg.lo = __builtin_amdgcn_readfirstlane(*(LAS u32*)(L + L_TAG + 4));
    if (h < 4) {
      f32x4 acc = {0.f, 0.f, 0.f, 0.f};
#pragma unroll
      for (int ks = 0; ks < 4; ks++) {
        const u32x4 af = *(LAS u32x4*)(L + L_XN + (n & 1) * 256 + ks * 64 + g * 16);
        int r0 = 32 * ks + 8 * g + q4;
        if (ks == 3) r0 = (g >= 2) ? r0 - 16 : r0;
        const u32 a = lds0 + L_WQ + r0 * 128 + 32 * h + 8 * p4;
        acc = mfma16(af, tr_frag(a, a + 4 * 128), acc);
      }
      if (g < 2) put(ws + G_QP + (g * 4 + kq) * 4096 + 64 * cg + 16 * h + n, g ? acc[1] : acc[0], tg);
      STAMP(4);
    }
    cnt_wait(cnt + C_KT, 8);
    float qg[4];
    sweep<4>(ws + G_QP + b * 4 * 4096 + kvh * 512 + 64 * h + l, 4096, tg, qg);
    *(LAS u16*)(L + L_Q + (64 * h + l) * 2) = f2bf((qg[0] + qg[1]) + (qg[2] + qg[3]));
    cnt_add(cnt0 + 4 * C_Q);
    STAMP(5);
  }
  {
    cnt_wait(cnt + C_Q, 8);
    u32x4 qf[8];
#pragma unroll
    for (int j = 0; j < 8; j++) qf[j] = *(LAS u32x4*)(L + L_Q + (n & 1) * 512 + j * 64 + g * 16);
    cnt_wait(cnt + C_K, 8);
    STAMP(6);
    const LAS unsigned char* kr = L + L_K + (32 * h + n) * 512;
    u32x4 kf[16];
#pragma unroll
    for (int j = 0; j < 8; j++) {
      kf[2 * j] = *(const LAS u32x4*)(kr + (((4 * j + g) ^ n) * 16));
      kf[2 * j + 1] = *(const LAS u32x4*)(kr + 8192 + (((4 * j + g) ^ n) * 16));
    }
    __builtin_amdgcn_sched_barrier(0);
    f32x4 acc0 = {0.f, 0.f, 0.f, 0.f}, acc1 = {0.f, 0.f, 0.f, 0.f};
#pragma unroll
    for (int j = 0; j < 8; j++) { acc0 = mfma16(qf[j], kf[2 * j], acc0); acc1 = mfma16(qf[j], kf[2 * j + 1], acc1); }
    const float sr = (g & 1) ? ((g >> 1) ? acc1[1] : acc1[0]) : ((g >> 1) ? acc0[1] : acc0[0]);
    const float sc = rbf(sr) * 0.0625f;
    const float mw = x16_max(row_max(sc));
    LAS float* sm = (LAS float*)(L + L_SM);
    if ((l & 31) == 0) sm[(g >> 1) * 8 + h] = mw;
    cnt_add(cnt0 + 4 * C_MAX);
    cnt_spin(cnt + C_MAX, 8);
    const f32x4 ma = *(LAS f32x4*)(sm + (g >> 1) * 8), mb = *(LAS f32x4*)(sm + (g >> 1) * 8 + 4);
    *(LAS u32x4*)(L + L_RED + h * 2048 + l * 32) = u32x4{~0u, ~0u, ~0u, ~0u};
    *(LAS u32x4*)(L + L_RED + h * 2048 + l * 32 + 16) = u32x4{~0u, ~0u, ~0u, ~0u};
    const float m = fmaxf(fmaxf(fmaxf(ma[0], ma[1]), fmaxf(ma[2], ma[3])), fmaxf(fmaxf(mb[0], mb[1]), fmaxf(mb[2], mb[3])));
    const u16 pb = f2bf(__builtin_amdgcn_exp2f((sc - m) * 1.4426950408889634f));
    const int row = 16 * (g & 1) + n;
    *(LAS u16*)(L + L_P + (g >> 1) * 512 + (row >> 2) * 64 + (row & 3) * 16 + ((h - s) & 7) * 2) = pb;
    const float lw = x16_sum(row_sum(bflo((u32)pb)));
    if ((l & 31) == 0) sm[16 + (g >> 1) * 8 + h] = lw;
    cnt_add(cnt0 + 4 * C_P);
    STAMP(7);
    if (h == 7) {
      cnt_spin(cnt + C_P, 8);
      const f32x4 la = *(LAS f32x4*)(sm + 16 + (g >> 1) * 8), lb = *(LAS f32x4*)(sm + 16 + (g >> 1) * 8 + 4);
      const float ls = ((la[0] + la[1]) + (la[2] + la[3])) + ((lb[0] + lb[1]) + (lb[2] + lb[3]));
      if ((l & 30) == 0) put(ws + G_S1 + (((kvh * 2 + (g >> 1)) * 2 + b) * 16 + s) * 2 + (l & 1), (l & 1) ? ls : m, tg);
    }
  }
  const Tag tgo{tg.hi, tg.lo ^ 2u};
  const u32 tgy = tg.hi ^ 0x5bd1e995u;
  const f32x4 z4 = {0.f, 0.f, 0.f, 0.f};
  constexpr u32 SLO = 0x05040100u, SHI = 0x07060302u;
  if (idx < 16) {
    const int r1 = (idx >> 2) * 8 + xcd, cq = idx & 3, cb = 2 * cq + h;
    const bool mm = h < 2 && cb < 7;
    u32x2 wr[4][8];
    if (mm) {
      cnt_wait(cnt + C_VISS, WAT);
      if (WOPV) cnt_wait(cnt + C_PV, WOPV);
      if (WODONE) cnt_wait(cnt + C_DONE, 8);
      const unsigned char* wsrc = (const unsigned char*)P.wo + (size_t)(r1 * 128 + 8 * g) * 896 + 128 * cb + 8 * n;
#pragma unroll
      for (int ks = 0; ks < 4; ks++)
#pragma unroll
        for (int j = 0; j < 8; j++)
          asm volatile("global_load_dwordx2 %0, %1, off offset:%2" POLW : "=v"(wr[ks][j]) : "v"(wsrc + ks * 32 * 896 + (j >> 2) * 3584), "i"((j & 3) * 896) : "memory");
    }
    const gu64* pd = ws + G_P1 + (4 * r1 + (h >> 1)) * 1024 + (h & 1) * 512 + 2 * l;
    const gu64* ps = ws + G_S1 + ((r1 >> 1) * 2 + (h & 1)) * 32 + (l & 31);
    u32x4 wf[4][4];
    if (mm) {
#pragma unroll
      for (int ks = 0; ks < 4; ks++) {
        asm volatile("s_waitcnt vmcnt(%8)" : "+v"(wr[ks][0]), "+v"(wr[ks][1]), "+v"(wr[ks][2]), "+v"(wr[ks][3]), "+v"(wr[ks][4]), "+v"(wr[ks][5]),
                     "+v"(wr[ks][6]), "+v"(wr[ks][7]) : "i"(24 - 8 * ks) : "memory");
#pragma unroll
        for (int e = 0; e < 4; e++)
#pragma unroll
          for (int t = 0; t < 4; t++) wf[ks][e][t] = __builtin_amdgcn_perm(wr[ks][2 * t + 1][e >> 1], wr[ks][2 * t][e >> 1], (e & 1) ? SHI : SLO);
#pragma unroll
        for (int e = 0; e < 4; e++) asm volatile("" : "+v"(wf[ks][e]));
      }
    }
    GAS u32x4* dst = (GAS u32x4*)(ws + G_Y + r1 * 512 + (g & 1) * 224 + 32 * cb + 2 * n);
    const LAS u32x4* ap = (const LAS u32x4*)(L + L_OB + (n & 1) * 256 + g * 16);
    asm volatile("" : "+v"(dst), "+v"(ap));
    STAMP(8);
    cnt_wait(cnt + C_DONE, 8);
    STAMP(2);
    u32x4 pv[4];
    u64 sg;
    float wk[4];
    u32 pend = 0x1fu;
    for (int spins = 0; spins < 4 * SPIN_CAP && pend; spins++) {
#pragma unroll
      for (int k = 0; k < 4; k++) if (pend & (1u << k))
        asm volatile("global_load_dwordx4 %0, %1, off offset:%2 sc1" : "=v"(pv[k]) : "v"(pd), "i"(k * 1024) : "memory");
      if (pend & 0x10u) asm volatile("global_load_dwordx2 %0, %1, off sc1" : "=v"(sg) : "v"(ps) : "memory");
      asm volatile("s_waitcnt vmcnt(0)" : "+v"(pv[0]), "+v"(pv[1]), "+v"(pv[2]), "+v"(pv[3]), "+v"(sg) :: "memory");
#pragma unroll
      for (int k = 0; k < 4; k++) if (pend & (1u << k)) {
        if (__all(((pv[k][1] ^ tg.hi) | (pv[k][3] ^ tg.hi) | (((pv[k][0] ^ tg.lo) | (pv[k][2] ^ tg.lo)) & 0xffu)) == 0u)) pend &= ~(1u << k);
      }
      if ((pend & 0x10u) && __all(tag_bad(sg, tg) == 0u)) {
        pend &= 0xfu;
        const float st = dec(sg);
        const float M = x16_max(row_max((l & 1) ? -INFINITY : st));
        const float wgt = __builtin_amdgcn_exp2f((st - M) * 1.4426950408889634f);
        const float lw = dpp<0xB1>(wgt) * st;
        const float wn = wgt * __builtin_amdgcn_rcpf(x16_sum(row_sum((l & 1) ? lw : 0.f)));
#pragma unroll
        for (int k = 0; k < 4; k++) wk[k] = asf(__builtin_amdgcn_ds_bpermute(8 * (4 * k + g), asu(wn)));
      }
    }
    STAMP(9);
    {
      float o0 = 0.f, o1 = 0.f;
#pragma unroll
      for (int k = 0; k < 4; k++) { o0 = fmaf(wk[k], asf(pv[k][0] & 0xffffff00u), o0); o1 = fmaf(wk[k], asf(pv[k][2] & 0xffffff00u), o1); }
      o0 = x32_sum(x16_sum(o0)); o1 = x32_sum(x16_sum(o1));
      if (l < 16) *(LAS u32*)(L + L_OB + (h & 1) * 256 + (h >> 1) * 64 + n * 4) = pk2bf(o0, o1);
    }
    wg_barrier();
    if (mm) {
      u32x4 af[4];
#pragma unroll
      for (int ks = 0; ks < 4; ks++) af[ks] = ap[4 * ks];
      f32x4 d[4] = {z4, z4, z4, z4};
#pragma unroll
      for (int ks = 0; ks < 4; ks++)
#pragma unroll
        for (int e = 0; e < 4; e++) d[e] = mfma16(af[ks], wf[ks][e], d[e]);
      const u32 lo = (g & 1) ? pk2bf(d[0][1], d[1][1]) : pk2bf(d[0][0], d[1][0]);
      const u32 hi = (g & 1) ? pk2bf(d[2][1], d[3][1]) : pk2bf(d[2][0], d[3][0]);
      const u32x4 val = {lo, tgy, hi, tgy};
      if (YREP == 1) {
        if (g < 2) asm volatile("global_store_dwordx4 %0, %1, off sc1\n\ts_nop 1" :: "v"(dst), "v"(val) : "memory");
      } else {
#pragma unroll
        for (int c2 = 0; c2 < (YREP + 1) / 2; c2++)
          if (2 * c2 + 1 < YREP || g < 2)
            asm volatile("global_store_dwordx4 %0, %1, off sc1\n\ts_nop 1" :: "v"(dst + (2 * c2 + (g >> 1)) * (32 * 256)), "v"(val) : "memory");
      }
      STAMP(10);
    }
    if (h == 7 || cq < 2 || r1 >= 28) { FLUSH(); return; }
    const int t = 2 * r1 + cq - 2, tb = t >= 28 ? 1 : 0, col = 16 * t - 448 * tb;
    u32 hpk = 0;
    for (int spins = 0; spins < SPIN_CAP; spins++) {
      const u64 gg = __hip_atomic_load(ws + G_H + 8 * t + (n >> 1), RLX_AGENT);
      hpk = (u32)gg;
      if (__all((u32)(gg >> 32) == tgy)) break;
      __builtin_amdgcn_s_sleep(SLOWS);
    }
    STAMP(17);
    float fv[2];
    sweep<2>(ws + G_P1 + (tb * 56 + 8 * h + g) * 1024 + 512 + col + n, 4096, tgo, fv);
    STAMP(11);
    const float y = x32_sum(x16_sum(fv[0] + fv[1]));
    if (l < 16) *(LAS float*)(L + L_UR + h * 64 + n * 4) = y;
    wg_barrier();
    if (h == 0) {
      float yy = 0.f;
#pragma unroll
      for (int k = 0; k < 7; k++) yy += *(LAS float*)(L + L_UR + k * 64 + n * 4);
      if (l < 16) P.out[16 * t + n] = f2bf(((n & 1) ? bfhi(hpk) : bflo(hpk)) + rbf(yy));
      if (t == 0 && l == 0) __hip_atomic_store(ws + G_EPOCH, *(LAS u64*)(L + L_TAG + 8), RLX_AGENT);
      STAMP(12);
    }
  } else if (xcd < 7) {
    if (h == 7) { FLUSH(); return; }
    const int fs = xcd * 8 + ((idx - 16) >> 1), fb = idx & 1;
    u32 w1r[2][8];
    u32x2 w2r[8];
    cnt_wait(cnt + C_VISS, WAT);
    if (WDEFER == 2) cnt_wait(cnt + C_DONE, 8);
    if (WDEFER == 3) cnt_wait(cnt + C_PV, FPV);
    {
      const unsigned char* s1 = (const unsigned char*)P.w1 + (size_t)(64 * h + 8 * g) * 3584 + 64 * fs + 4 * n;
      const unsigned char* s2 = (const unsigned char*)P.w2 + (size_t)(32 * fs + 8 * g) * 896 + 128 * h + 8 * n;
#pragma unroll
      for (int a = 0; a < 2; a++)
#pragma unroll
        for (int j = 0; j < 8; j++) asm volatile("global_load_dword %0, %1, off" POLF : "=v"(w1r[a][j]) : "v"(s1 + (32 * a + j) * 3584) : "memory");
#pragma unroll
      for (int j = 0; j < 8; j++)
        asm volatile("global_load_dwordx2 %0, %1, off offset:%2" POLF : "=v"(w2r[j]) : "v"(s2 + (j >> 2) * 3584), "i"((j & 3) * 896) : "memory");
    }
    const u32x2 xw = *(const u32x2*)(P.x + fb * 448 + 64 * h + 4 * n);
    asm volatile("s_waitcnt vmcnt(0)" : "+v"(w1r[0][0]), "+v"(w1r[0][1]), "+v"(w1r[0][2]), "+v"(w1r[0][3]), "+v"(w1r[0][4]), "+v"(w1r[0][5]), "+v"(w1r[0][6]),
                 "+v"(w1r[0][7]), "+v"(w1r[1][0]), "+v"(w1r[1][1]), "+v"(w1r[1][2]), "+v"(w1r[1][3]), "+v"(w1r[1][4]), "+v"(w1r[1][5]), "+v"(w1r[1][6]),
                 "+v"(w1r[1][7]) :: "memory");
    asm volatile("" : "+v"(w2r[0]), "+v"(w2r[1]), "+v"(w2r[2]), "+v"(w2r[3]), "+v"(w2r[4]), "+v"(w2r[5]), "+v"(w2r[6]), "+v"(w2r[7]) :: "memory");
    u32x4 w1f[2][2], w2f[4];
#pragma unroll
    for (int t = 0; t < 4; t++) {
#pragma unroll
      for (int a = 0; a < 2; a++) {
        w1f[a][0][t] = __builtin_amdgcn_perm(w1r[a][2 * t + 1], w1r[a][2 * t], SLO);
        w1f[a][1][t] = __builtin_amdgcn_perm(w1r[a][2 * t + 1], w1r[a][2 * t], SHI);
      }
#pragma unroll
      for (int e = 0; e < 4; e++) w2f[e][t] = __builtin_amdgcn_perm(w2r[2 * t + 1][e >> 1], w2r[2 * t][e >> 1], (e & 1) ? SHI : SLO);
    }
    const u32x4 ones = {0x3f803f80u, 0x3f803f80u, 0x3f803f80u, 0x3f803f80u};
    float cs;
    {
      f32x4 c0 = mfma16(ones, w1f[0][0], z4), c1 = mfma16(ones, w1f[0][1], z4);
      c0 = mfma16(ones, w1f[1][0], c0); c1 = mfma16(ones, w1f[1][1], c1);
      if (g < 2) *(LAS float*)(L + L_UR + h * 160 + (2 * n + g) * 4) = g ? c1[0] : c0[0];
      wg_barrier();
      cs = 0.f;
#pragma unroll
      for (int k = 0; k < 7; k++) cs += *(LAS float*)(L + L_UR + k * 160 + (l & 31) * 4);
      wg_barrier();
    }
    gu64* dst = ws + G_P1 + (fb * 56 + fs) * 1024 + 512 + 64 * h + 4 * n + g;
    const gu64* src = ws + G_Y + (xcd % YREP) * (32 * 512) + g * 512 + fb * 224 + 32 * h + 2 * n;
    asm volatile("" : "+v"(dst), "+v"(src), "+v"(cs));
#pragma unroll
    for (int e = 0; e < 4; e++) asm volatile("" : "+v"(w2f[e]));
    asm volatile("" : "+v"(w1f[0][0]), "+v"(w1f[0][1]), "+v"(w1f[1][0]), "+v"(w1f[1][1]));
    STAMP(13);
    for (int spins = 0; FSLOW && spins < SPIN_CAP; spins++) {
      if (__any((u32)(__hip_atomic_load(src + h * 2048, RLX_AGENT) >> 32) == tgy)) break;
      __builtin_amdgcn_s_sleep(SLOWS);
    }
    u32x4 yv[8];
    {
      u32 pend = 0xffu;
      for (int spins = 0; spins < 4 * SPIN_CAP && pend; spins++) {
#pragma unroll
        for (int k = 0; k < 8; k++) if (pend & (1u << k))
          asm volatile("global_load_dwordx4 %0, %1, off sc1" : "=v"(yv[k]) : "v"(src + k * 2048) : "memory");
        asm volatile("s_waitcnt vmcnt(0)" : "+v"(yv[0]), "+v"(yv[1]), "+v"(yv[2]), "+v"(yv[3]), "+v"(yv[4]), "+v"(yv[5]), "+v"(yv[6]), "+v"(yv[7]) :: "memory");
#pragma unroll
        for (int k = 0; k < 8; k++) if (pend & (1u << k)) {
          if (__all(((yv[k][1] ^ tgy) | (yv[k][3] ^ tgy)) == 0u)) pend &= ~(1u << k);
        }
      }
    }
    STAMP(16);
    u32x2 hp;
    {
      float y[4];
#pragma unroll
      for (int e = 0; e < 4; e++) {
        u32x4 bq;
#pragma unroll
        for (int t = 0; t < 4; t++) bq[t] = __builtin_amdgcn_perm(yv[2 * t + 1][e & 2], yv[2 * t][e & 2], (e & 1) ? SHI : SLO);
        y[e] = mfma16(ones, bq, z4)[0];
      }
      hp.x = pk2bf(bflo(xw.x) + rbf(y[0]), bfhi(xw.x) + rbf(y[1]));
      hp.y = pk2bf(bflo(xw.y) + rbf(y[2]), bfhi(xw.y) + rbf(y[3]));
      if (l < 16) *(LAS u32x2*)(L + L_FB + h * 128 + n * 8) = hp;
    }
    waitlgkm();
    const u32x4 af0 = *(LAS u32x4*)(L + L_FB + h * 128 + g * 16), af1 = *(LAS u32x4*)(L + L_FB + h * 128 + 64 + g * 16);
    if (fs == 0 && l < 32) __hip_atomic_store(ws + G_H + fb * 224 + 32 * h + 2 * n + g, ((u64)tgy << 32) | (g ? hp.y : hp.x), RLX_AGENT);
    STAMP(14);
    f32x4 u0, u1, st;
    {
      const u32x4 b0 = n == 0 ? af0 : ones, b1 = n == 0 ? af1 : ones;
      u0 = mfma16(af0, w1f[0][0], z4); u1 = mfma16(af0, w1f[0][1], z4); st = mfma16(af0, b0, z4);
      u0 = mfma16(af1, w1f[1][0], u0); u1 = mfma16(af1, w1f[1][1], u1); st = mfma16(af1, b1, st);
    }
    {
      const float uv = (g == 0) ? u0[0] : (g == 1) ? u1[0] : st[0];
      if (g < 2 || l < 34) *(LAS float*)(L + L_UR + h * 160 + (g < 2 ? 2 * n + g : 32 + n) * 4) = uv;
    }
    wg_barrier();
    {
      float ua = 0.f;
      f32x2 sa = {0.f, 0.f};
#pragma unroll
      for (int k = 0; k < 7; k++) {
        ua += *(LAS float*)(L + L_UR + k * 160 + (l & 31) * 4);
        sa += *(LAS f32x2*)(L + L_UR + k * 160 + 128);
      }
      const float mean = sa[1] * (1.f / 448.f);
      const float var = rbf(fmaxf(fmaf(-mean, mean, sa[0] * (1.f / 448.f)), 0.f));
      const float rden = __builtin_amdgcn_rcpf(rbf(__builtin_amdgcn_sqrtf(rbf(var + 1e-5f))));
      const float u = rbf(fmaf(-rbf(mean), cs, ua) * rden);
      const float f = u * __builtin_amdgcn_rcpf(1.f + __builtin_amdgcn_exp2f(-u * 1.4426950408889634f));
      if (l < 32) *(LAS u16*)(L + L_FB + h * 128 + l * 2) = f2bf(f);
    }
    waitlgkm();
    const u32x4 ff = *(LAS u32x4*)(L + L_FB + h * 128 + g * 16);
    f32x4 d[4];
#pragma unroll
    for (int e = 0; e < 4; e++) d[e] = mfma16(ff, w2f[e], z4);
    put(dst, (g >> 1) ? ((g & 1) ? d[3][0] : d[2][0]) : ((g & 1) ? d[1][0] : d[0][0]), tgo);
    STAMP(15);
  }
  FLUSH();
}

extern "C" void kernel_launch(void* const* d_in, const int* in_sizes, int n_in, void* d_out, int out_size, void* d_ws, size_t ws_size,
                              hipStream_t stream) {
  Params p;
  p.x = (const u16*)d_in[0]; p.kc = (const u16*)d_in[1]; p.vc = (const u16*)d_in[2]; p.wq = (const u16*)d_in[3]; p.wo = (const u16*)d_in[4];
  p.w1 = (const u16*)d_in[6]; p.w2 = (const u16*)d_in[7]; p.out = (u16*)d_out; p.ws = (u64*)d_ws;
  fused_block_kernel<<<256, NT, 0, stream>>>(p);
}
```

```cpp
#define EV -1
#define PRETOUCH 0
#include <hip/hip_runtime.h>
#include <stdint.h>

typedef unsigned short u16;
typedef unsigned u32;
typedef unsigned long long u64;
typedef u32 u32x4 __attribute__((ext_vector_type(4)));
typedef u32 u32x2 __attribute__((ext_vector_type(2)));
typedef float f32x4 __attribute__((ext_vector_type(4)));
typedef float f32x2 __attribute__((ext_vector_type(2)));
typedef __bf16 bf16x8 __attribute__((ext_vector_type(8)));
typedef __bf16 bf16x2 __attribute__((ext_vector_type(2)));
#define GAS __attribute__((address_space(1)))
#define LAS __attribute__((address_space(3)))
typedef GAS u64 gu64;
#define RLX_AGENT __ATOMIC_RELAXED, __HIP_MEMORY_SCOPE_AGENT
#define SC "sc1"
#define RLX_WG __ATOMIC_RELAXED, __HIP_MEMORY_SCOPE_WORKGROUP

#define WIN 4
#define WDEFER 2
#define WODEFER 1
#define QTRIG 6
#define OPENW 1
#define XCDMAP 0
#define SLOWS 4
#define NOSTREAM 0
#define FSLOW 0
#define FPV 2
#define YREP 1
#define WOPV 0
#define WODONE 0
#define KFIRST -1
#define P1BF 1
#define SWAPR 1
#define PV16 1
#define FDMA 1
#define PSL 0
#define YSL 0
#define WAT 8
#define G_BASE 0
static_assert(QTRIG + WIN - 2 < 16, "trigger inside the K loop");

constexpr int NT = 1024;
constexpr int SPIN_CAP = 1 << 16;
constexpr int G_EPOCH = 0;
constexpr int G_QP = 512;
constexpr int G_P1 = G_QP + 32768;
constexpr int OPB = P1BF ? 512 : 1024, OPO = P1BF ? 0 : 512;
constexpr int G_S1 = G_P1 + 128 * OPB;
constexpr int G_H = G_S1 + 1024;
constexpr int G_Y = G_H + 512;
constexpr int G_END = G_Y + YREP * 32 * 512;
constexpr int G_STAMP = 1 << 20;
constexpr int L_K = 0;
constexpr int L_RED = 65536;
constexpr int L_WQ = 131072;
constexpr int L_XN = L_WQ + 14336;
constexpr int L_Q = L_XN + 512;
constexpr int L_P = L_Q + 1024;
constexpr int L_SM = L_P + 1024;
constexpr int L_CNT = L_SM + 128;
constexpr int L_TAG = L_CNT + 64;
constexpr int L_OB = L_TAG + 16;
constexpr int L_UR = L_OB + 512;
constexpr int L_FB = L_UR + 8 * 320;
constexpr int L_END = L_FB + 1024;
enum { C_Q = 0, C_K, C_WQ, C_KT, C_MAX, C_P, C_VISS, C_DONE, C_W, C_PV, C_N };

struct Params {
  const u16* x; const u16* kc; const u16* vc; const u16* wq; const u16* wo; const u16* w1; const u16* w2; u16* out; u64* ws;
};

__device__ __forceinline__ float asf(u32 u) { return __builtin_bit_cast(float, u); }
__device__ __forceinline__ u32 asu(float f) { return __builtin_bit_cast(u32, f); }
__device__ __forceinline__ float bflo(u32 u) { return asf(u << 16); }
__device__ __forceinline__ float bfhi(u32 u) { return asf(u & 0xffff0000u); }
__device__ __forceinline__ u16 f2bf(float f) { return __builtin_bit_cast(u16, (__bf16)f); }
__device__ __forceinline__ float rbf(float f) { return asf((u32)f2bf(f) << 16); }
__device__ __forceinline__ u32 pk2bf(float lo, float hi) { bf16x2 v = {(__bf16)lo, (__bf16)hi}; return __builtin_bit_cast(u32, v); }
template <int CTRL> __device__ __forceinline__ float dpp(float x) {
  return __builtin_bit_cast(float, __builtin_amdgcn_mov_dpp(__builtin_bit_cast(int, x), CTRL, 0xf, 0xf, true));
}
__device__ __forceinline__ float x16_sum(float x) { auto s = __builtin_amdgcn_permlane16_swap(asu(x), asu(x), false, false); return asf(s[0]) + asf(s[1]); }
__device__ __forceinline__ float x32_sum(float x) { auto s = __builtin_amdgcn_permlane32_swap(asu(x), asu(x), false, false); return asf(s[0]) + asf(s[1]); }
__device__ __forceinline__ float x16_max(float x) { auto s = __builtin_amdgcn_permlane16_swap(asu(x), asu(x), false, false); return fmaxf(asf(s[0]), asf(s[1])); }
__device__ __forceinline__ float row_sum(float x) { x += dpp<0xB1>(x); x += dpp<0x4E>(x); x += dpp<0x141>(x); x += dpp<0x128>(x); return x; }
__device__ __forceinline__ float row_max(float x) {
  x = fmaxf(x, dpp<0xB1>(x)); x = fmaxf(x, dpp<0x4E>(x)); x = fmaxf(x, dpp<0x141>(x)); x = fmaxf(x, dpp<0x128>(x)); return x;
}
__device__ __forceinline__ float wave_sum(float x) { return x32_sum(x16_sum(row_sum(x))); }
__device__ __forceinline__ f32x4 mfma16(u32x4 a, u32x4 b, f32x4 c) {
  return __builtin_amdgcn_mfma_f32_16x16x32_bf16(__builtin_bit_cast(bf16x8, a), __builtin_bit_cast(bf16x8, b), c, 0, 0, 0);
}
typedef short s16x4 __attribute__((ext_vector_type(4)));
__device__ __forceinline__ f32x4 mfma16k16(u32x2 a, u32x2 b, f32x4 c) {
  return __builtin_amdgcn_mfma_f32_16x16x16bf16_1k(__builtin_bit_cast(s16x4, a), __builtin_bit_cast(s16x4, b), c, 0, 0, 0);
}
template <int N> __device__ __forceinline__ void waitvm() { asm volatile("s_waitcnt vmcnt(%0)" ::"i"(N) : "memory"); }
__device__ __forceinline__ void waitlgkm() { asm volatile("s_waitcnt lgkmcnt(0)" ::: "memory"); }
__device__ __forceinline__ void wg_barrier() { asm volatile("s_waitcnt lgkmcnt(0)\n\ts_barrier" ::: "memory"); }
#define POLKV " nt"
#define POLV POLKV
#define POLF " nt"
#define POLW " nt"
__device__ __forceinline__ void glds16(const void* sbase, u32 voff, u32 lds_dst) {
  u32 keep;
  asm volatile("s_mov_b32 %0, m0\n\ts_mov_b32 m0, %3\n\ts_nop 0\n\tglobal_load_lds_dwordx4 %1, %2" POLW "\n\ts_mov_b32 m0, %0"
               : "=&s"(keep) : "v"(voff), "s"(sbase), "s"(lds_dst) : "memory");
}
__device__ __forceinline__ void glds16k(const void* sbase, u32 voff, u32 lds_dst) {
  u32 keep;
  asm volatile("s_mov_b32 %0, m0\n\ts_mov_b32 m0, %3\n\ts_nop 0\n\tglobal_load_lds_dwordx4 %1, %2" POLKV "\n\ts_mov_b32 m0, %0"
               : "=&s"(keep) : "v"(voff), "s"(sbase), "s"(lds_dst) : "memory");
}
__device__ __forceinline__ u32x4 tr_frag(u32 a0, u32 a1) {
  u32x2 r0, r1;
  asm volatile("ds_read_b64_tr_b16 %0, %2\n\tds_read_b64_tr_b16 %1, %3\n\ts_waitcnt lgkmcnt(0)" : "=&v"(r0), "=&v"(r1) : "v"(a0), "v"(a1) : "memory");
  return u32x4{r0.x, r0.y, r1.x, r1.y};
}

struct Tag { u32 hi, lo; };
__device__ __forceinline__ void put(gu64* p, float v, Tag t) {
  __hip_atomic_store(p, ((u64)t.hi << 32) | (u64)((asu(v) & 0xffffff00u) | t.lo), RLX_AGENT);
}
__device__ __forceinline__ u32 tag_bad(u64 g, Tag t) { return (((u32)(g >> 32)) ^ t.hi) | (((u32)g ^ t.lo) & 0xffu); }
__device__ __forceinline__ float dec(u64 g) { return asf((u32)g & 0xffffff00u); }
template <int N> __device__ __forceinline__ void sweep(const gu64* p, int S, Tag t, float (&v)[N]) {
  for (int spins = 0; spins < SPIN_CAP; spins++) {
    u64 g[N];
    u32 bh = 0, bl = 0;
#pragma unroll
    for (int k = 0; k < N; k++) g[k] = __hip_atomic_load(p + k * S, RLX_AGENT);
#pragma unroll
    for (int k = 0; k < N; k++) { bh |= (u32)(g[k] >> 32) ^ t.hi; bl |= (u32)g[k] ^ t.lo; v[k] = dec(g[k]); }
    if (__all((bh | (bl & 0xffu)) == 0u)) break;
  }
}
__device__ __forceinline__ void slow_start(const gu64* p, Tag t) {
  for (int spins = 0; spins < SPIN_CAP; spins++) {
    if (__any(tag_bad(__hip_atomic_load(p, RLX_AGENT), t) == 0u)) break;
    __builtin_amdgcn_s_sleep(SLOWS);
  }
}

__device__ __forceinline__ void cnt_add(u32 addr) {
  u64 keep;
  asm volatile("s_waitcnt lgkmcnt(0)\n\ts_mov_b64 %0, exec\n\ts_mov_b64 exec, 1\n\tds_add_u32 %1, %2\n\ts_mov_b64 exec, %0"
               : "=&s"(keep) : "v"(addr), "v"(1u) : "memory");
}
__device__ __forceinline__ void cnt_wait(LAS u32* c, u32 n) {
  while (__hip_atomic_load(c, RLX_WG) < n) __builtin_amdgcn_s_sleep(1);
  asm volatile("" ::: "memory");
}
__device__ __forceinline__ void cnt_spin(LAS u32* c, u32 n) {
  while (__hip_atomic_load(c, RLX_WG) < n) {}
  asm volatile("" ::: "memory");
}

#define STAMP(i) do { if ((i) == EV) { const u32 t_ = (u32)__builtin_amdgcn_s_memrealtime(); if (l == 0) __hip_atomic_store((gu64*)P.ws + G_END + 1024 + wg * 16 + wave, ((u64)*(LAS u32*)(L + L_TAG) << 32) | t_, RLX_AGENT); } } while (0)
#define FLUSH() do {} while (0)

__global__ void __launch_bounds__(NT, 4) fused_block_kernel(Params P) {
  __shared__ __attribute__((aligned(16))) unsigned char smem[L_END];
  LAS unsigned char* L = (LAS unsigned char*)smem;
  const u32 lds0 = (u32)(size_t)smem;
  const u64 t_start = __builtin_amdgcn_s_memrealtime();
  const int wg = blockIdx.x;
  const int wave = __builtin_amdgcn_readfirstlane(threadIdx.x >> 6), l = threadIdx.x & 63;
  const int n = l & 15, g = l >> 4;
  const int xcd = wg & 7, idx = (wg >> 3) ^ (SWAPR ? 16 : 0);
  const int kvh = (wg >> 4) & 7, b = wg >> 7, s = wg & 15;
  const int pair = b * 8 + kvh, aidx = b * 16 + s;
  gu64* ws = (gu64*)P.ws + G_BASE;
  LAS u32* cnt = (LAS u32*)(L + L_CNT);
  const u32 cnt0 = lds0 + L_CNT;

  if (wave < 8) {
    const int w = wave, hf = l >> 5, c = l & 31;
    if (threadIdx.x < 16) cnt[threadIdx.x] = 0u;
    STAMP(0);
    const unsigned char* kb = (const unsigned char*)P.kc + ((size_t)pair * 4096 + s * 256) * 512;
    const unsigned char* vb = (const unsigned char*)P.vc + ((size_t)pair * 4096 + s * 256) * 512;
    const int wr = (w + b) & 7;
    const u32 koff = hf * 512 + ((c ^ ((2 * wr + hf) & 15)) * 16);
    if (KFIRST < 0) wg_barrier();
#pragma unroll
    for (int i = 0; i < 16; i++) {
      const int gc = ((i + s) & 15) * 8 + wr;
      glds16k(kb + gc * 1024, koff, lds0 + L_K + gc * 1024);
      if (i == KFIRST) wg_barrier();
      if (i >= WIN - 1) waitvm<WIN - 1>();
      if (i == QTRIG + WIN - 2) cnt_add(cnt0 + 4 * C_KT);
    }
    u32x4 v[16];
    const u32 voff = g * 512 + n * 16;
    bool open = false;
#pragma unroll
    for (int i = 0; i < 16; i++) {
      const int gc2 = (((i >> 1) + s) & 7) * 8 + wr;
      asm volatile("global_load_dwordx4 %0, %1, %2 offset:%3" POLV : "=v"(v[i]) : "v"(voff), "s"(vb + gc2 * 2048), "i"((i & 1) * 256) : "memory");
      if (i < 15 && !open) waitvm<WIN - 1>();
      if (i == WIN - 2) cnt_add(cnt0 + 4 * C_K);
      if (OPENW && i >= WIN - 2 && i < 15 && !open) {
        const u64 qk = __hip_atomic_load((LAS u64*)(cnt + C_Q), RLX_WG);
        open = __builtin_amdgcn_readfirstlane((u32)qk + (u32)(qk >> 32)) >= 16;
      }
    }
    cnt_add(cnt0 + 4 * C_VISS);
    STAMP(1);
    cnt_wait(cnt + C_P, 8);
    STAMP(18);
    const u32x4 pf = *(LAS u32x4*)(L + L_P + (n >> 3) * 512 + wr * 64 + g * 16);
    const u32x4 zero4 = {0, 0, 0, 0};
    const u32x4 pa0 = (n & 4) ? zero4 : pf, pa1 = (n & 4) ? pf : zero4;
    const Tag tg{*(LAS u32*)(L + L_TAG), *(LAS u32*)(L + L_TAG + 4)};
    gu64* dst = ws + G_P1 + ((kvh * 16 + 2 * w + hf) * 2 + b) * 256 + s * 16 + (c >> 1);
    asm volatile("" : "+v"(dst));
    f32x4 d[8];
#pragma unroll
    for (int e = 0; e < 8; e++) d[e] = f32x4{0.f, 0.f, 0.f, 0.f};
#pragma unroll
    for (int hk = 0; hk < 2; hk++) {
      u32x2 bf0[8], bf1[8];
#pragma unroll
      for (int t2 = 0; t2 < 2; t2++) {
        const int t = 2 * hk + t2;
        asm volatile("s_waitcnt vmcnt(%4)" : "+v"(v[4 * t]), "+v"(v[4 * t + 1]), "+v"(v[4 * t + 2]), "+v"(v[4 * t + 3]) : "i"(12 - 4 * t) : "memory");
#pragma unroll
        for (int e = 0; e < 8; e++) {
          const u32 sel = (e & 1) ? 0x07060302u : 0x05040100u;
          bf0[e][t2] = __builtin_amdgcn_perm(v[4 * t + 2][e >> 1], v[4 * t][e >> 1], sel);
          bf1[e][t2] = __builtin_amdgcn_perm(v[4 * t + 3][e >> 1], v[4 * t + 1][e >> 1], sel);
        }
        if (t2 == 0) {
#pragma unroll
          for (int e = 0; e < 8; e++) asm volatile("" : "+v"(bf0[e]), "+v"(bf1[e]));
        }
      }
      const u32x2 a0 = {pa0[2 * hk], pa0[2 * hk + 1]}, a1 = {pa1[2 * hk], pa1[2 * hk + 1]};
#pragma unroll
      for (int e = 0; e < 8; e++) d[e] = mfma16k16(a0, bf0[e], d[e]);
#pragma unroll
      for (int e = 0; e < 8; e++) d[e] = mfma16k16(a1, bf1[e], d[e]);
      if (hk == 0) {
#pragma unroll
        for (int e = 0; e < 8; e++) asm volatile("" : "+v"(d[e]));
      }
    }
    *(LAS f32x4*)(L + L_RED + w * 2048 + l * 32) = f32x4{d[0][0], d[1][0], d[2][0], d[3][0]};
    *(LAS f32x4*)(L + L_RED + w * 2048 + l * 32 + 16) = f32x4{d[4][0], d[5][0], d[6][0], d[7][0]};
    if (WOPV || WDEFER == 3) cnt_add(cnt0 + 4 * C_PV);
    STAMP(19);
    float o = 0.f;
    for (int spins = 0; spins < (1 << 18); spins++) {
      u32 x[8];
      asm volatile("" ::: "memory");
#pragma unroll
      for (int ww = 0; ww < 8; ww++) x[ww] = *(volatile LAS u32*)(L + L_RED + ww * 2048 + (64 * w + l) * 4);
      const u32 mx = max(max(max(x[0], x[1]), max(x[2], x[3])), max(max(x[4], x[5]), max(x[6], x[7])));
      o = ((asf(x[0]) + asf(x[1])) + (asf(x[2]) + asf(x[3]))) + ((asf(x[4]) + asf(x[5])) + (asf(x[6]) + asf(x[7])));
      if (__all(mx != 0xffffffffu)) break;
    }
    {
      const u32 pk = pk2bf(o, dpp<0xB1>(o));
      if (!(l & 1)) __hip_atomic_store(dst, ((u64)(tg.hi ^ 0x2545f491u) << 32) | pk, RLX_AGENT);
    }
    cnt_add(cnt0 + 4 * C_DONE);
    STAMP(3);
    FLUSH();
    return;
  }

  const int h = wave - 8, q4 = n >> 2, p4 = n & 3;
  Tag tg;
  {
    const int cg = 8 * kvh + (aidx >> 2), kq = aidx & 3;
    const unsigned char* wqs = (const unsigned char*)P.wq + (size_t)(112 * kq) * 8192 + 128 * cg;
    if (h < 7) {
#pragma unroll
      for (int d = 0; d < 2; d++) glds16(wqs + (size_t)(16 * h + 8 * d) * 8192, (l >> 3) * 8192 + (l & 7) * 16, lds0 + L_WQ + (2 * h + d) * 1024);
    }
    u32x4 xr = {0, 0, 0, 0};
    if (h == 5 || h == 6) xr = *(const u32x4*)(P.x + (h & 1) * 448 + (l < 56 ? l : 55) * 8);
    if (h == 7) {
      u64 ep;
      u32 junk;
      const gu64* epp = ws + G_EPOCH;
      const unsigned char* tp = (const unsigned char*)P.out;
      const int rr = xcd * 16 + idx, fs = xcd * 8 + (idx - 16);
      if (l == 1) tp = (const unsigned char*)P.vc + ((size_t)pair * 4096 + s * 256) * 512;
      if (l == 2) tp = (const unsigned char*)P.vc + ((size_t)pair * 4096 + s * 256 + 255) * 512;
      if (l == 3) tp = (const unsigned char*)P.kc + ((size_t)pair * 4096 + s * 256 + 255) * 512;
      if (l == 4) tp = (const unsigned char*)(ws + G_P1 + kvh * 16384);
      if (l == 5) tp = (const unsigned char*)(ws + G_P1 + 131071);
      if (l == 6) tp = (const unsigned char*)(ws + G_END - 1);
      if (idx < 16) {
        if (l == 7) tp = (const unsigned char*)P.wo + (size_t)rr * 32 * 896;
        if (l == 8) tp = (const unsigned char*)P.wo + (size_t)rr * 32 * 896 + 28668;
      } else if (idx < 24 && xcd < 7) {
        if (l == 7) tp = (const unsigned char*)P.w1 + fs * 64;
        if (l == 8) tp = (const unsigned char*)P.w1 + (size_t)447 * 3584 + fs * 64;
        if (l == 9) tp = (const unsigned char*)P.w1 + (size_t)224 * 3584 + fs * 64;
        if (l == 10) tp = (const unsigned char*)P.w2 + (size_t)fs * 32 * 896;
        if (l == 11) tp = (const unsigned char*)P.w2 + (size_t)fs * 32 * 896 + 28668;
      }
      asm volatile("global_load_dwordx2 %0, %1, off " SC : "=v"(ep) : "v"(epp) : "memory");
      wg_barrier();
      asm volatile("s_waitcnt vmcnt(0)" : "+v"(ep) :: "memory");
      const u64 ne = ep * 6364136223846793005ull + 1442695040888963407ull;
      if (l == 0) __hip_atomic_store(ws + G_END + 64 + wg, ((u64)(u32)(ne >> 8) << 32) | (u32)t_start, RLX_AGENT);
      if (l == 0) *(LAS u32x4*)(L + L_TAG) = u32x4{(u32)(ne >> 8), (u32)ne & 0xffu, (u32)ne, (u32)(ne >> 32)};
      cnt_add(cnt0 + 4 * C_WQ);
    } else {
      wg_barrier();
      if (h >= 5) {
        float xv[8];
#pragma unroll
        for (int k = 0; k < 4; k++) { xv[2 * k] = bflo(xr[k]); xv[2 * k + 1] = bfhi(xr[k]); }
        float sm = ((xv[0] + xv[1]) + (xv[2] + xv[3])) + ((xv[4] + xv[5]) + (xv[6] + xv[7]));
        const float mean = wave_sum(l < 56 ? sm : 0.f) * (1.f / 448.f);
        float ss = 0.f;
#pragma unroll
        for (int k = 0; k < 8; k++) { const float d = xv[k] - mean; ss = fmaf(d, d, ss); }
        const float var = rbf(wave_sum(l < 56 ? ss : 0.f) * (1.f / 448.f)), mb = rbf(mean);
        const float rden = __builtin_amdgcn_rcpf(rbf(__builtin_amdgcn_sqrtf(rbf(var + 1e-5f))));
        u32x4 o;
#pragma unroll
        for (int k = 0; k < 4; k++) o[k] = pk2bf(rbf(xv[2 * k] - mb) * rden, rbf(xv[2 * k + 1] - mb) * rden);
        const int rl = l - 14 * kq;
        if (rl >= 0 && rl < 14) *(LAS u32x4*)(L + L_XN + (h & 1) * 256 + rl * 16) = o;
        if (l >= 62) *(LAS u32x4*)(L + L_XN + (h & 1) * 256 + (l - 48) * 16) = u32x4{0, 0, 0, 0};
      }
      waitvm<0>();
      cnt_add(cnt0 + 4 * C_WQ);
    }
    cnt_spin(cnt + C_WQ, 8);
    tg.hi = __builtin_amdgcn_readfirstlane(*(LAS u32*)(L + L_TAG));
    tg.lo = __builtin_amdgcn_readfirstlane(*(LAS u32*)(L + L_TAG + 4));
    if (h < 4) {
      f32x4 acc = {0.f, 0.f, 0.f, 0.f};
#pragma unroll
      for (int ks = 0; ks < 4; ks++) {
        const u32x4 af = *(LAS u32x4*)(L + L_XN + (n & 1) * 256 + ks * 64 + g * 16);
        int r0 = 32 * ks + 8 * g + q4;
        if (ks == 3) r0 = (g >= 2) ? r0 - 16 : r0;
        const u32 a = lds0 + L_WQ + r0 * 128 + 32 * h + 8 * p4;
        acc = mfma16(af, tr_frag(a, a + 4 * 128), acc);
      }
      if (g < 2) put(ws + G_QP + (g * 4 + kq) * 4096 + 64 * cg + 16 * h + n, g ? acc[1] : acc[0], tg);
      STAMP(4);
    }
    cnt_wait(cnt + C_KT, 8);
    float qg[4];
    sweep<4>(ws + G_QP + b * 4 * 4096 + kvh * 512 + 64 * h + l, 4096, tg, qg);
    *(LAS u16*)(L + L_Q + (64 * h + l) * 2) = f2bf((qg[0] + qg[1]) + (qg[2] + qg[3]));
    cnt_add(cnt0 + 4 * C_Q);
    STAMP(5);
  }
  {
    cnt_wait(cnt + C_Q, 8);
    u32x4 qf[8];
#pragma unroll
    for (int j = 0; j < 8; j++) qf[j] = *(LAS u32x4*)(L + L_Q + (n & 1) * 512 + j * 64 + g * 16);
    cnt_wait(cnt + C_K, 8);
    STAMP(6);
    const LAS unsigned char* kr = L + L_K + (32 * h + n) * 512;
    u32x4 kf[16];
#pragma unroll
    for (int j = 0; j < 8; j++) {
      kf[2 * j] = *(const LAS u32x4*)(kr + (((4 * j + g) ^ n) * 16));
      kf[2 * j + 1] = *(const LAS u32x4*)(kr + 8192 + (((4 * j + g) ^ n) * 16));
    }
    __builtin_amdgcn_sched_barrier(0);
    f32x4 acc0 = {0.f, 0.f, 0.f, 0.f}, acc1 = {0.f, 0.f, 0.f, 0.f};
#pragma unroll
    for (int j = 0; j < 8; j++) { acc0 = mfma16(qf[j], kf[2 * j], acc0); acc1 = mfma16(qf[j], kf[2 * j + 1], acc1); }
    const float sr = (g & 1) ? ((g >> 1) ? acc1[1] : acc1[0]) : ((g >> 1) ? acc0[1] : acc0[0]);
    const float sc = rbf(sr) * 0.0625f;
    const float mw = x16_max(row_max(sc));
    LAS float* sm = (LAS float*)(L + L_SM);
    if ((l & 31) == 0) sm[(g >> 1) * 8 + h] = mw;
    cnt_add(cnt0 + 4 * C_MAX);
    cnt_spin(cnt + C_MAX, 8);
    const f32x4 ma = *(LAS f32x4*)(sm + (g >> 1) * 8), mb = *(LAS f32x4*)(sm + (g >> 1) * 8 + 4);
    *(LAS u32x4*)(L + L_RED + h * 2048 + l * 32) = u32x4{~0u, ~0u, ~0u, ~0u};
    *(LAS u32x4*)(L + L_RED + h * 2048 + l * 32 + 16) = u32x4{~0u, ~0u, ~0u, ~0u};
    const float m = fmaxf(fmaxf(fmaxf(ma[0], ma[1]), fmaxf(ma[2], ma[3])), fmaxf(fmaxf(mb[0], mb[1]), fmaxf(mb[2], mb[3])));
    const u16 pb = f2bf(__builtin_amdgcn_exp2f((sc - m) * 1.4426950408889634f));
    const int row = 16 * (g & 1) + n;
    *(LAS u16*)(L + L_P + (g >> 1) * 512 + (row >> 2) * 64 + (row & 3) * 16 + ((h - s) & 7) * 2) = pb;
    const float lw = x16_sum(row_sum(bflo((u32)pb)));
    if ((l & 31) == 0) sm[16 + (g >> 1) * 8 + h] = lw;
    cnt_add(cnt0 + 4 * C_P);
    STAMP(7);
    if (h == 7) {
      cnt_spin(cnt + C_P, 8);
      const f32x4 la = *(LAS f32x4*)(sm + 16 + (g >> 1) * 8), lb = *(LAS f32x4*)(sm + 16 + (g >> 1) * 8 + 4);
      const float ls = ((la[0] + la[1]) + (la[2] + la[3])) + ((lb[0] + lb[1]) + (lb[2] + lb[3]));
      if ((l & 30) == 0) put(ws + G_S1 + (((kvh * 2 + (g >> 1)) * 2 + b) * 16 + s) * 2 + (l & 1), (l & 1) ? ls : m, tg);
    }
  }
  const Tag tgo{tg.hi, tg.lo ^ 2u};
  const u32 tgy = tg.hi ^ 0x5bd1e995u;
  const f32x4 z4 = {0.f, 0.f, 0.f, 0.f};
  constexpr u32 SLO = 0x05040100u, SHI = 0x07060302u;
  if (idx < 16) {
    const int r1 = (idx >> 2) * 8 + xcd, cq = idx & 3, cb = 2 * cq + h;
    const bool mm = h < 2 && cb < 7;
    u32x2 wr[4][8];
    if (mm) {
      cnt_wait(cnt + C_VISS, WAT);
      if (WOPV) cnt_wait(cnt + C_PV, WOPV);
      if (WODONE) cnt_wait(cnt + C_DONE, 8);
      const unsigned char* wsrc = (const unsigned char*)P.wo + (size_t)(r1 * 128 + 8 * g) * 896 + 128 * cb + 8 * n;
#pragma unroll
      for (int ks = 0; ks < 4; ks++)
#pragma unroll
        for (int j = 0; j < 8; j++)
          asm volatile("global_load_dwordx2 %0, %1, off offset:%2" POLW : "=v"(wr[ks][j]) : "v"(wsrc + ks * 32 * 896 + (j >> 2) * 3584), "i"((j & 3) * 896) : "memory");
    }
    const gu64* pd = ws + G_P1 + (4 * r1 + (h >> 1)) * 512 + (h & 1) * 256 + 2 * l;
    constexpr int NP = 2;
    const u32 tgp = tg.hi ^ 0x2545f491u;
    const gu64* ps = ws + G_S1 + ((r1 >> 1) * 2 + (h & 1)) * 32 + (l & 31);
    u32x4 wf[4][4];
    if (mm) {
#pragma unroll
      for (int ks = 0; ks < 4; ks++) {
        asm volatile("s_waitcnt vmcnt(%8)" : "+v"(wr[ks][0]), "+v"(wr[ks][1]), "+v"(wr[ks][2]), "+v"(wr[ks][3]), "+v"(wr[ks][4]), "+v"(wr[ks][5]),
                     "+v"(wr[ks][6]), "+v"(wr[ks][7]) : "i"(24 - 8 * ks) : "memory");
#pragma unroll
        for (int e = 0; e < 4; e++)
#pragma unroll
          for (int t = 0; t < 4; t++) wf[ks][e][t] = __builtin_amdgcn_perm(wr[ks][2 * t + 1][e >> 1], wr[ks][2 * t][e >> 1], (e & 1) ? SHI : SLO);
#pragma unroll
        for (int e = 0; e < 4; e++) asm volatile("" : "+v"(wf[ks][e]));
      }
    }
    GAS u32x4* dst = (GAS u32x4*)(ws + G_Y + r1 * 512 + (g & 1) * 224 + 32 * cb + 2 * n);
    const LAS u32x4* ap = (const LAS u32x4*)(L + L_OB + (n & 1) * 256 + g * 16);
    asm volatile("" : "+v"(dst), "+v"(ap));
    STAMP(8);
    cnt_wait(cnt + C_DONE, 8);
    STAMP(2);
    u32x4 pv[4];
    u64 sg;
    float wk[4];
    u32 pend = 0x10u | ((1u << NP) - 1u);
    for (int spins = 0; spins < 4 * SPIN_CAP && pend; spins++) {
#pragma unroll
      for (int k = 0; k < NP; k++) if (pend & (1u << k))
        asm volatile("global_load_dwordx4 %0, %1, off offset:%2 " SC : "=v"(pv[k]) : "v"(pd), "i"(k * 1024) : "memory");
      if (pend & 0x10u) asm volatile("global_load_dwordx2 %0, %1, off " SC : "=v"(sg) : "v"(ps) : "memory");
      asm volatile("s_waitcnt vmcnt(0)" : "+v"(pv[0]), "+v"(pv[1]), "+v"(sg) :: "memory");
#pragma unroll
      for (int k = 0; k < NP; k++) if (pend & (1u << k)) {
        if (__all(((pv[k][1] ^ tgp) | (pv[k][3] ^ tgp)) == 0u)) pend &= ~(1u << k);
      }
      if ((pend & 0x10u) && __all(tag_bad(sg, tg) == 0u)) {
        pend &= 0xfu;
        const float st = dec(sg);
        const float M = x16_max(row_max((l & 1) ? -INFINITY : st));
        const float wgt = __builtin_amdgcn_exp2f((st - M) * 1.4426950408889634f);
        const float lw = dpp<0xB1>(wgt) * st;
        const float wn = wgt * __builtin_amdgcn_rcpf(x16_sum(row_sum((l & 1) ? lw : 0.f)));
#pragma unroll
        for (int k = 0; k < NP; k++) wk[k] = asf(__builtin_amdgcn_ds_bpermute(8 * (P1BF ? 8 * k + (l >> 3) : 4 * k + g), asu(wn)));
      }
      if (PSL && pend) __builtin_amdgcn_s_sleep(PSL);
    }
    STAMP(9);
    {
      float o[4];
      o[0] = fmaf(wk[1], bflo(pv[1][0]), wk[0] * bflo(pv[0][0])); o[1] = fmaf(wk[1], bfhi(pv[1][0]), wk[0] * bfhi(pv[0][0]));
      o[2] = fmaf(wk[1], bflo(pv[1][2]), wk[0] * bflo(pv[0][2])); o[3] = fmaf(wk[1], bfhi(pv[1][2]), wk[0] * bfhi(pv[0][2]));
#pragma unroll
      for (int e = 0; e < 4; e++) { o[e] += dpp<0x128>(o[e]); o[e] = x32_sum(x16_sum(o[e])); }
      if (l < 8) *(LAS u32x2*)(L + L_OB + (h & 1) * 256 + (h >> 1) * 64 + l * 8) = u32x2{pk2bf(o[0], o[1]), pk2bf(o[2], o[3])};
    }
    wg_barrier();
    if (mm) {
      u32x4 af[4];
#pragma unroll
      for (int ks = 0; ks < 4; ks++) af[ks] = ap[4 * ks];
      f32x4 d[4] = {z4, z4, z4, z4};
#pragma unroll
      for (int ks = 0; ks < 4; ks++)
#pragma unroll
        for (int e = 0; e < 4; e++) d[e] = mfma16(af[ks], wf[ks][e], d[e]);
      const u32 lo = (g & 1) ? pk2bf(d[0][1], d[1][1]) : pk2bf(d[0][0], d[1][0]);
      const u32 hi = (g & 1) ? pk2bf(d[2][1], d[3][1]) : pk2bf(d[2][0], d[3][0]);
      const u32x4 val = {lo, tgy, hi, tgy};
      if (YREP == 1) {
        if (g < 2) asm volatile("global_store_dwordx4 %0, %1, off " SC "\n\ts_nop 1" :: "v"(dst), "v"(val) : "memory");
      } else {
#pragma unroll
        for (int c2 = 0; c2 < (YREP + 1) / 2; c2++)
          if (2 * c2 + 1 < YREP || g < 2)
            asm volatile("global_store_dwordx4 %0, %1, off " SC "\n\ts_nop 1" :: "v"(dst + (2 * c2 + (g >> 1)) * (32 * 256)), "v"(val) : "memory");
      }
      STAMP(10);
    }
    if (h == 7 || cq < 2 || r1 >= 28) { FLUSH(); return; }
    const int t = 2 * r1 + cq - 2, tb = t >= 28 ? 1 : 0, col = 16 * t - 448 * tb;
    u32 hpk = 0;
    for (int spins = 0; spins < SPIN_CAP; spins++) {
      const u64 gg = __hip_atomic_load(ws + G_H + 8 * t + (n >> 1), RLX_AGENT);
      hpk = (u32)gg;
      if (__all((u32)(gg >> 32) == tgy)) break;
      __builtin_amdgcn_s_sleep(SLOWS);
    }
    STAMP(17);
    float fv[2];
    sweep<2>(ws + G_P1 + (tb * 56 + 8 * h + g) * OPB + OPO + col + n, 4 * OPB, tgo, fv);
    STAMP(11);
    const float y = x32_sum(x16_sum(fv[0] + fv[1]));
    if (l < 16) *(LAS float*)(L + L_UR + h * 64 + n * 4) = y;
    wg_barrier();
    if (h == 0) {
      float yy = 0.f;
#pragma unroll
      for (int k = 0; k < 7; k++) yy += *(LAS float*)(L + L_UR + k * 64 + n * 4);
      if (t == 0) {
        const u32 now = (u32)__builtin_amdgcn_s_memrealtime();
        u32 mx = 0, mn = 0xffffffffu;
        for (int k = 0; k < 4; k++) { const u32 age = now - (u32)__hip_atomic_load(ws + G_END + 64 + k * 64 + l, RLX_AGENT); mx = max(mx, age); }
        for (int k = 0; k < 64; k++) { const u64 e = __hip_atomic_load(ws + G_END + 1024 + k * 64 + l, RLX_AGENT); if ((u32)(e >> 32) == tg.hi) mn = min(mn, now - (u32)e); }
        for (int o = 32; o > 0; o >>= 1) { mx = max(mx, (u32)__shfl_xor((int)mx, o)); mn = min(mn, (u32)__shfl_xor((int)mn, o)); }
        const u32 delay = EV < 0 ? 0 : (mx - min(mn, mx)) * 4;
        const u32 t1 = (u32)__builtin_amdgcn_s_memrealtime();
        while ((u32)__builtin_amdgcn_s_memrealtime() - t1 < min(delay, 100000u)) __builtin_amdgcn_s_sleep(2);
      }
      if (l < 16) P.out[16 * t + n] = f2bf(((n & 1) ? bfhi(hpk) : bflo(hpk)) + rbf(yy));
      if (t == 0 && l == 0) __hip_atomic_store(ws + G_EPOCH, *(LAS u64*)(L + L_TAG + 8), RLX_AGENT);
      STAMP(12);
    }
  } else if (xcd < 7) {
    if (h == 7) { FLUSH(); return; }
    const int fs = xcd * 8 + ((idx - 16) >> 1), fb = idx & 1;
    cnt_wait(cnt + C_DONE, 8);
    {
      const unsigned char* w1s = (const unsigned char*)P.w1 + fs * 64;
      const unsigned char* w2s = (const unsigned char*)P.w2 + (size_t)fs * 32 * 896;
#pragma unroll
      for (int d = 0; d < 4; d++) glds16(w1s + (size_t)(h * 4 + d) * 16 * 3584, (l >> 2) * 3584 + (l & 3) * 16, lds0 + L_K + (h * 4 + d) * 1024);
#pragma unroll
      for (int d = 0; d < 4; d++) glds16(w2s + (h * 4 + d) * 1024, l * 16, lds0 + L_K + 28672 + (h * 4 + d) * 1024);
    }
    const u32x2 xw = *(const u32x2*)(P.x + fb * 448 + 64 * h + 4 * n);
    waitvm<0>();
    cnt_add(cnt0 + 4 * C_W);
    u32x4 w1f[2][2], w2f[4];
    {
      const u32 a1 = lds0 + L_K + (64 * h + 8 * g + q4) * 64 + 8 * p4;
      u32x2 r[8];
      asm volatile("ds_read_b64_tr_b16 %0, %8\n\tds_read_b64_tr_b16 %1, %8 offset:256\n\tds_read_b64_tr_b16 %2, %8 offset:32\n\tds_read_b64_tr_b16 %3, %8 offset:288\n\t"
                   "ds_read_b64_tr_b16 %4, %8 offset:2048\n\tds_read_b64_tr_b16 %5, %8 offset:2304\n\tds_read_b64_tr_b16 %6, %8 offset:2080\n\tds_read_b64_tr_b16 %7, %8 offset:2336\n\t"
                   "s_waitcnt lgkmcnt(0)"
                   : "=&v"(r[0]), "=&v"(r[1]), "=&v"(r[2]), "=&v"(r[3]), "=&v"(r[4]), "=&v"(r[5]), "=&v"(r[6]), "=&v"(r[7]) : "v"(a1) : "memory");
#pragma unroll
      for (int k = 0; k < 4; k++) w1f[k >> 1][k & 1] = u32x4{r[2 * k].x, r[2 * k].y, r[2 * k + 1].x, r[2 * k + 1].y};
      cnt_spin(cnt + C_W, 7);
      const u32 a2 = lds0 + L_K + 28672 + (8 * g + q4) * 896 + 128 * h + 8 * p4;
      asm volatile("ds_read_b64_tr_b16 %0, %8\n\tds_read_b64_tr_b16 %1, %8 offset:3584\n\tds_read_b64_tr_b16 %2, %8 offset:32\n\tds_read_b64_tr_b16 %3, %8 offset:3616\n\t"
                   "ds_read_b64_tr_b16 %4, %8 offset:64\n\tds_read_b64_tr_b16 %5, %8 offset:3648\n\tds_read_b64_tr_b16 %6, %8 offset:96\n\tds_read_b64_tr_b16 %7, %8 offset:3680\n\t"
                   "s_waitcnt lgkmcnt(0)"
                   : "=&v"(r[0]), "=&v"(r[1]), "=&v"(r[2]), "=&v"(r[3]), "=&v"(r[4]), "=&v"(r[5]), "=&v"(r[6]), "=&v"(r[7]) : "v"(a2) : "memory");
#pragma unroll
      for (int k = 0; k < 4; k++) w2f[k] = u32x4{r[2 * k].x, r[2 * k].y, r[2 * k + 1].x, r[2 * k + 1].y};
    }
    const u32x4 ones = {0x3f803f80u, 0x3f803f80u, 0x3f803f80u, 0x3f803f80u};
    gu64* dst = ws + G_P1 + (fb * 56 + fs) * OPB + OPO + 64 * h + (FDMA ? l : 4 * n + g);
    const gu64* src = ws + G_Y + (xcd % YREP) * (32 * 512) + g * 512 + fb * 224 + 32 * h + 2 * n;
    asm volatile("" : "+v"(dst), "+v"(src));
#pragma unroll
    for (int e = 0; e < 4; e++) asm volatile("" : "+v"(w2f[e]));
    asm volatile("" : "+v"(w1f[0][0]), "+v"(w1f[0][1]), "+v"(w1f[1][0]), "+v"(w1f[1][1]));
    STAMP(13);
    for (int spins = 0; FSLOW && spins < SPIN_CAP; spins++) {
      if (__any((u32)(__hip_atomic_load(src + h * 2048, RLX_AGENT) >> 32) == tgy)) break;
      __builtin_amdgcn_s_sleep(SLOWS);
    }
    u32x4 yv[8];
    {
      u32 pend = 0xffu;
      for (int spins = 0; spins < 4 * SPIN_CAP && pend; spins++) {
#pragma unroll
        for (int k = 0; k < 8; k++) if (pend & (1u << k))
          asm volatile("global_load_dwordx4 %0, %1, off " SC : "=v"(yv[k]) : "v"(src + k * 2048) : "memory");
        asm volatile("s_waitcnt vmcnt(0)" : "+v"(yv[0]), "+v"(yv[1]), "+v"(yv[2]), "+v"(yv[3]), "+v"(yv[4]), "+v"(yv[5]), "+v"(yv[6]), "+v"(yv[7]) :: "memory");
#pragma unroll
        for (int k = 0; k < 8; k++) if (pend & (1u << k)) {
          if (__all(((yv[k][1] ^ tgy) | (yv[k][3] ^ tgy)) == 0u)) pend &= ~(1u << k);
        }
        if (YSL && pend) __builtin_amdgcn_s_sleep(YSL);
      }
    }
    STAMP(16);
    u32x2 hp;
    {
      float y[4];
#pragma unroll
      for (int e = 0; e < 4; e++) {
        u32x4 bq;
#pragma unroll
        for (int t = 0; t < 4; t++) bq[t] = __builtin_amdgcn_perm(yv[2 * t + 1][e & 2], yv[2 * t][e & 2], (e & 1) ? SHI : SLO);
        y[e] = mfma16(ones, bq, z4)[0];
      }
      hp.x = pk2bf(bflo(xw.x) + rbf(y[0]), bfhi(xw.x) + rbf(y[1]));
      hp.y = pk2bf(bflo(xw.y) + rbf(y[2]), bfhi(xw.y) + rbf(y[3]));
      if (l < 16) *(LAS u32x2*)(L + L_FB + h * 128 + n * 8) = hp;
    }
    waitlgkm();
    const u32x4 af0 = *(LAS u32x4*)(L + L_FB + h * 128 + g * 16), af1 = *(LAS u32x4*)(L + L_FB + h * 128 + 64 + g * 16);
    if (fs == 0 && l < 32) __hip_atomic_store(ws + G_H + fb * 224 + 32 * h + 2 * n + g, ((u64)tgy << 32) | (g ? hp.y : hp.x), RLX_AGENT);
    STAMP(14);
    f32x4 u0, u1, st;
    {
      const u32x4 a0 = (n & 1) ? ones : af0, a1 = (n & 1) ? ones : af1;
      u0 = mfma16(a0, w1f[0][0], z4); u1 = mfma16(a0, w1f[0][1], z4); st = mfma16(a0, af0, z4);
      u0 = mfma16(a1, w1f[1][0], u0); u1 = mfma16(a1, w1f[1][1], u1); st = mfma16(a1, af1, st);
    }
    {
      const f32x2 uv = (g == 0) ? f32x2{u0[0], u0[1]} : (g == 1) ? f32x2{u1[0], u1[1]} : f32x2{st[0], st[1]};
      if (l < 33) *(LAS f32x2*)(L + L_UR + h * 320 + (FDMA ? l : (g < 2 ? 2 * n + g : 32)) * 8) = uv;
    }
    wg_barrier();
    {
      f32x2 uc = {0.f, 0.f}, sa = {0.f, 0.f};
#pragma unroll
      for (int k = 0; k < 7; k++) {
        uc += *(LAS f32x2*)(L + L_UR + k * 320 + (l & 31) * 8);
        sa += *(LAS f32x2*)(L + L_UR + k * 320 + 256);
      }
      const float ua = uc[0], cs = uc[1];
      const float mean = sa[1] * (1.f / 448.f);
      const float var = rbf(fmaxf(fmaf(-mean, mean, sa[0] * (1.f / 448.f)), 0.f));
      const float rden = __builtin_amdgcn_rcpf(rbf(__builtin_amdgcn_sqrtf(rbf(var + 1e-5f))));
      const float u = rbf(fmaf(-rbf(mean), cs, ua) * rden);
      const float f = u * __builtin_amdgcn_rcpf(1.f + __builtin_amdgcn_exp2f(-u * 1.4426950408889634f));
      if (l < 32) *(LAS u16*)(L + L_FB + h * 128 + l * 2) = f2bf(f);
    }
    waitlgkm();
    const u32x4 ff = *(LAS u32x4*)(L + L_FB + h * 128 + g * 16);
    f32x4 d[4];
#pragma unroll
    for (int e = 0; e < 4; e++) d[e] = mfma16(ff, w2f[e], z4);
    put(dst, (g >> 1) ? ((g & 1) ? d[3][0] : d[2][0]) : ((g & 1) ? d[1][0] : d[0][0]), tgo);
    STAMP(15);
  }
  FLUSH();
}

extern "C" void kernel_launch(void* const* d_in, const int* in_sizes, int n_in, void* d_out, int out_size, void* d_ws, size_t ws_size,
                              hipStream_t stream) {
  Params p;
  p.x = (const u16*)d_in[0]; p.kc = (const u16*)d_in[1]; p.vc = (const u16*)d_in[2]; p.wq = (const u16*)d_in[3]; p.wo = (const u16*)d_in[4];
  p.w1 = (const u16*)d_in[6]; p.w2 = (const u16*)d_in[7]; p.out = (u16*)d_out; p.ws = (u64*)d_ws;
  fused_block_kernel<<<256, NT, 0, stream>>>(p);
}
```

```cpp
#include <hip/hip_runtime.h>
#include <stdint.h>

typedef unsigned short u16;
typedef unsigned u32;
typedef unsigned long long u64;
typedef u32 u32x4 __attribute__((ext_vector_type(4)));
typedef u32 u32x2 __attribute__((ext_vector_type(2)));
typedef float f32x4 __attribute__((ext_vector_type(4)));
typedef float f32x2 __attribute__((ext_vector_type(2)));
typedef __bf16 bf16x8 __attribute__((ext_vector_type(8)));
typedef __bf16 bf16x2 __attribute__((ext_vector_type(2)));
#define GAS __attribute__((address_space(1)))
#define LAS __attribute__((address_space(3)))
typedef GAS u64 gu64;
#define RLX_AGENT __ATOMIC_RELAXED, __HIP_MEMORY_SCOPE_AGENT
#define RLX_WG __ATOMIC_RELAXED, __HIP_MEMORY_SCOPE_WORKGROUP

constexpr int WIN = 4;
constexpr int QTRIG = 6;
static_assert(QTRIG + WIN - 2 < 16, "trigger inside the K loop");

constexpr int NT = 1024;
constexpr int SPIN_CAP = 1 << 16;
constexpr int G_EPOCH = 0;
constexpr int G_QP = 512;
constexpr int G_P1 = G_QP + 32768;
constexpr int OPB = 512;
constexpr int G_S1 = G_P1 + 128 * OPB;
constexpr int G_H = G_S1 + 1024;
constexpr int G_END = G_H + 512;
constexpr int L_K = 0;
constexpr int L_RED = 65536;
constexpr int L_WQ = 131072;
constexpr int L_XN = L_WQ + 14336;
constexpr int L_Q = L_XN + 512;
constexpr int L_P = L_Q + 1024;
constexpr int L_SM = L_P + 1024;
constexpr int L_CNT = L_SM + 128;
constexpr int L_TAG = L_CNT + 64;
constexpr int L_OB = L_TAG + 16;
constexpr int L_UR = L_OB + 512;
constexpr int L_FB = L_UR + 8 * 320;
constexpr int L_END = L_FB + 1024;
enum { C_Q = 0, C_K, C_WQ, C_KT, C_MAX, C_P, C_VISS, C_DONE, C_W, C_N };

struct Params {
  const u16* x; const u16* kc; const u16* vc; const u16* wq; const u16* wo; const u16* w1; const u16* w2; u16* out; u64* ws;
};

__device__ __forceinline__ float asf(u32 u) { return __builtin_bit_cast(float, u); }
__device__ __forceinline__ u32 asu(float f) { return __builtin_bit_cast(u32, f); }
__device__ __forceinline__ float bflo(u32 u) { return asf(u << 16); }
__device__ __forceinline__ float bfhi(u32 u) { return asf(u & 0xffff0000u); }
__device__ __forceinline__ u16 f2bf(float f) { return __builtin_bit_cast(u16, (__bf16)f); }
__device__ __forceinline__ float rbf(float f) { return asf((u32)f2bf(f) << 16); }
__device__ __forceinline__ u32 pk2bf(float lo, float hi) { bf16x2 v = {(__bf16)lo, (__bf16)hi}; return __builtin_bit_cast(u32, v); }
template <int CTRL> __device__ __forceinline__ float dpp(float x) {
  return __builtin_bit_cast(float, __builtin_amdgcn_mov_dpp(__builtin_bit_cast(int, x), CTRL, 0xf, 0xf, true));
}
__device__ __forceinline__ float x16_sum(float x) { auto s = __builtin_amdgcn_permlane16_swap(asu(x), asu(x), false, false); return asf(s[0]) + asf(s[1]); }
__device__ __forceinline__ float x32_sum(float x) { auto s = __builtin_amdgcn_permlane32_swap(asu(x), asu(x), false, false); return asf(s[0]) + asf(s[1]); }
__device__ __forceinline__ float x16_max(float x) { auto s = __builtin_amdgcn_permlane16_swap(asu(x), asu(x), false, false); return fmaxf(asf(s[0]), asf(s[1])); }
__device__ __forceinline__ float row_sum(float x) { x += dpp<0xB1>(x); x += dpp<0x4E>(x); x += dpp<0x141>(x); x += dpp<0x128>(x); return x; }
__device__ __forceinline__ float row_max(float x) {
  x = fmaxf(x, dpp<0xB1>(x)); x = fmaxf(x, dpp<0x4E>(x)); x = fmaxf(x, dpp<0x141>(x)); x = fmaxf(x, dpp<0x128>(x)); return x;
}
__device__ __forceinline__ float wave_sum(float x) { return x32_sum(x16_sum(row_sum(x))); }
__device__ __forceinline__ f32x4 mfma16(u32x4 a, u32x4 b, f32x4 c) {
  return __builtin_amdgcn_mfma_f32_16x16x32_bf16(__builtin_bit_cast(bf16x8, a), __builtin_bit_cast(bf16x8, b), c, 0, 0, 0);
}
typedef short s16x4 __attribute__((ext_vector_type(4)));
__device__ __forceinline__ f32x4 mfma16k16(u32x2 a, u32x2 b, f32x4 c) {
  return __builtin_amdgcn_mfma_f32_16x16x16bf16_1k(__builtin_bit_cast(s16x4, a), __builtin_bit_cast(s16x4, b), c, 0, 0, 0);
}
template <int N> __device__ __forceinline__ void waitvm() { asm volatile("s_waitcnt vmcnt(%0)" ::"i"(N) : "memory"); }
__device__ __forceinline__ void waitlgkm() { asm volatile("s_waitcnt lgkmcnt(0)" ::: "memory"); }
__device__ __forceinline__ void wg_barrier() { asm volatile("s_waitcnt lgkmcnt(0)\n\ts_barrier" ::: "memory"); }
__device__ __forceinline__ void glds16(const void* sbase, u32 voff, u32 lds_dst) {
  u32 keep;
  asm volatile("s_mov_b32 %0, m0\n\ts_mov_b32 m0, %3\n\ts_nop 0\n\tglobal_load_lds_dwordx4 %1, %2 nt\n\ts_mov_b32 m0, %0"
               : "=&s"(keep) : "v"(voff), "s"(sbase), "s"(lds_dst) : "memory");
}
__device__ __forceinline__ u32x4 tr_frag(u32 a0, u32 a1) {
  u32x2 r0, r1;
  asm volatile("ds_read_b64_tr_b16 %0, %2\n\tds_read_b64_tr_b16 %1, %3\n\ts_waitcnt lgkmcnt(0)" : "=&v"(r0), "=&v"(r1) : "v"(a0), "v"(a1) : "memory");
  return u32x4{r0.x, r0.y, r1.x, r1.y};
}

struct Tag { u32 hi, lo; };
__device__ __forceinline__ void put(gu64* p, float v, Tag t) {
  __hip_atomic_store(p, ((u64)t.hi << 32) | (u64)((asu(v) & 0xffffff00u) | t.lo), RLX_AGENT);
}
__device__ __forceinline__ u32 tag_bad(u64 g, Tag t) { return (((u32)(g >> 32)) ^ t.hi) | (((u32)g ^ t.lo) & 0xffu); }
__device__ __forceinline__ float dec(u64 g) { return asf((u32)g & 0xffffff00u); }
template <int N> __device__ __forceinline__ void sweep(const gu64* p, int S, Tag t, float (&v)[N]) {
  for (int spins = 0; spins < SPIN_CAP; spins++) {
    u64 g[N];
    u32 bh = 0, bl = 0;
#pragma unroll
    for (int k = 0; k < N; k++) g[k] = __hip_atomic_load(p + k * S, RLX_AGENT);
#pragma unroll
    for (int k = 0; k < N; k++) { bh |= (u32)(g[k] >> 32) ^ t.hi; bl |= (u32)g[k] ^ t.lo; v[k] = dec(g[k]); }
    if (__all((bh | (bl & 0xffu)) == 0u)) break;
  }
}
__device__ __forceinline__ void cnt_add(u32 addr) {
  u64 keep;
  asm volatile("s_waitcnt lgkmcnt(0)\n\ts_mov_b64 %0, exec\n\ts_mov_b64 exec, 1\n\tds_add_u32 %1, %2\n\ts_mov_b64 exec, %0"
               : "=&s"(keep) : "v"(addr), "v"(1u) : "memory");
}
__device__ __forceinline__ void cnt_wait(LAS u32* c, u32 n) {
  while (__hip_atomic_load(c, RLX_WG) < n) __builtin_amdgcn_s_sleep(1);
  asm volatile("" ::: "memory");
}
__device__ __forceinline__ void cnt_spin(LAS u32* c, u32 n) {
  while (__hip_atomic_load(c, RLX_WG) < n) {}
  asm volatile("" ::: "memory");
}

__global__ void __launch_bounds__(NT, 4) fused_block_kernel(Params P) {
  __shared__ __attribute__((aligned(128))) unsigned char smem[L_END];
  LAS unsigned char* L = (LAS unsigned char*)smem;
  const u32 lds0 = (u32)(size_t)smem;
  const int wg = blockIdx.x;
  const int wave = __builtin_amdgcn_readfirstlane(threadIdx.x >> 6), l = threadIdx.x & 63;
  const int n = l & 15, g = l >> 4;
  const int xcd = wg & 7, idx = (wg >> 3) ^ 16;
  const int kvh = (wg >> 4) & 7, b = wg >> 7, s = wg & 15;
  const int pair = b * 8 + kvh, aidx = b * 16 + s;
  gu64* ws = (gu64*)P.ws;
  LAS u32* cnt = (LAS u32*)(L + L_CNT);
  const u32 cnt0 = lds0 + L_CNT;

  if (wave < 8) {
    const int w = wave, hf = l >> 5, c = l & 31;
    if (threadIdx.x < 16) cnt[threadIdx.x] = 0u;
    const unsigned char* kb = (const unsigned char*)P.kc + ((size_t)pair * 4096 + s * 256) * 512;
    const unsigned char* vb = (const unsigned char*)P.vc + ((size_t)pair * 4096 + s * 256) * 512;
    const int wr = (w + b) & 7;
    const u32 koff = hf * 512 + ((c ^ ((2 * wr + hf) & 15)) * 16);
    wg_barrier();
#pragma unroll
    for (int i = 0; i < 16; i++) {
      const int gc = ((i + s) & 15) * 8 + wr;
      glds16(kb + gc * 1024, koff, lds0 + L_K + gc * 1024);
      if (i >= WIN - 1) waitvm<WIN - 1>();
      if (i == QTRIG + WIN - 2) cnt_add(cnt0 + 4 * C_KT);
    }
    u32x4 v[16];
    const u32 voff = g * 512 + n * 16;
    bool open = false;
#pragma unroll
    for (int i = 0; i < 16; i++) {
      const int gc2 = (((i >> 1) + s) & 7) * 8 + wr;
      asm volatile("global_load_dwordx4 %0, %1, %2 offset:%3 nt" : "=v"(v[i]) : "v"(voff), "s"(vb + gc2 * 2048), "i"((i & 1) * 256) : "memory");
      if (i < 15 && !open) waitvm<WIN - 1>();
      if (i == WIN - 2) cnt_add(cnt0 + 4 * C_K);
      if (i >= WIN - 2 && i < 15 && !open) {
        const u64 qk = __hip_atomic_load((LAS u64*)(cnt + C_Q), RLX_WG);
        open = __builtin_amdgcn_readfirstlane((u32)qk + (u32)(qk >> 32)) >= 16;
      }
    }
    cnt_add(cnt0 + 4 * C_VISS);
    cnt_wait(cnt + C_P, 8);
    const u32x4 pf = *(LAS u32x4*)(L + L_P + (n >> 3) * 512 + wr * 64 + g * 16);
    const u32x4 zero4 = {0, 0, 0, 0};
    const u32x4 pa0 = (n & 4) ? zero4 : pf, pa1 = (n & 4) ? pf : zero4;
    const Tag tg{*(LAS u32*)(L + L_TAG), *(LAS u32*)(L + L_TAG + 4)};
    gu64* dst = ws + G_P1 + ((kvh * 16 + 2 * w + hf) * 2 + b) * 256 + s * 16 + (c >> 1);
    asm volatile("" : "+v"(dst));
    f32x4 d[8];
#pragma unroll
    for (int e = 0; e < 8; e++) d[e] = f32x4{0.f, 0.f, 0.f, 0.f};
#pragma unroll
    for (int hk = 0; hk < 2; hk++) {
      u32x2 bf0[8], bf1[8];
#pragma unroll
      for (int t2 = 0; t2 < 2; t2++) {
        const int t = 2 * hk + t2;
        asm volatile("s_waitcnt vmcnt(%4)" : "+v"(v[4 * t]), "+v"(v[4 * t + 1]), "+v"(v[4 * t + 2]), "+v"(v[4 * t + 3]) : "i"(12 - 4 * t) : "memory");
#pragma unroll
        for (int e = 0; e < 8; e++) {
          const u32 sel = (e & 1) ? 0x07060302u : 0x05040100u;
          bf0[e][t2] = __builtin_amdgcn_perm(v[4 * t + 2][e >> 1], v[4 * t][e >> 1], sel);
          bf1[e][t2] = __builtin_amdgcn_perm(v[4 * t + 3][e >> 1], v[4 * t + 1][e >> 1], sel);
        }
        if (t2 == 0) {
#pragma unroll
          for (int e = 0; e < 8; e++) asm volatile("" : "+v"(bf0[e]), "+v"(bf1[e]));
        }
      }
      const u32x2 a0 = {pa0[2 * hk], pa0[2 * hk + 1]}, a1 = {pa1[2 * hk], pa1[2 * hk + 1]};
#pragma unroll
      for (int e = 0; e < 8; e++) d[e] = mfma16k16(a0, bf0[e], d[e]);
#pragma unroll
      for (int e = 0; e < 8; e++) d[e] = mfma16k16(a1, bf1[e], d[e]);
      if (hk == 0) {
#pragma unroll
        for (int e = 0; e < 8; e++) asm volatile("" : "+v"(d[e]));
      }
    }
    *(LAS f32x4*)(L + L_RED + w * 2048 + l * 32) = f32x4{d[0][0], d[1][0], d[2][0], d[3][0]};
    *(LAS f32x4*)(L + L_RED + w * 2048 + l * 32 + 16) = f32x4{d[4][0], d[5][0], d[6][0], d[7][0]};
    float o = 0.f;
    for (int spins = 0; spins < (1 << 18); spins++) {
      u32 x[8];
      asm volatile("" ::: "memory");
#pragma unroll
      for (int ww = 0; ww < 8; ww++) x[ww] = *(volatile LAS u32*)(L + L_RED + ww * 2048 + (64 * w + l) * 4);
      const u32 mx = max(max(max(x[0], x[1]), max(x[2], x[3])), max(max(x[4], x[5]), max(x[6], x[7])));
      o = ((asf(x[0]) + asf(x[1])) + (asf(x[2]) + asf(x[3]))) + ((asf(x[4]) + asf(x[5])) + (asf(x[6]) + asf(x[7])));
      if (__all(mx != 0xffffffffu)) break;
    }
    {
      const u32 pk = pk2bf(o, dpp<0xB1>(o));
      if (!(l & 1)) __hip_atomic_store(dst, ((u64)(tg.hi ^ 0x2545f491u) << 32) | pk, RLX_AGENT);
    }
    cnt_add(cnt0 + 4 * C_DONE);
    return;
  }

  const int h = wave - 8, q4 = n >> 2, p4 = n & 3;
  Tag tg;
  {
    const int cg = 8 * kvh + (aidx >> 2), kq = aidx & 3;
    const unsigned char* wqs = (const unsigned char*)P.wq + (size_t)(112 * kq) * 8192 + 128 * cg;
    if (h < 7) {
#pragma unroll
      for (int d = 0; d < 2; d++) glds16(wqs + (size_t)(16 * h + 8 * d) * 8192, (l >> 3) * 8192 + (l & 7) * 16, lds0 + L_WQ + (2 * h + d) * 1024);
    }
    u32x4 xr = {0, 0, 0, 0};
    if (h == 5 || h == 6) xr = *(const u32x4*)(P.x + (h & 1) * 448 + (l < 56 ? l : 55) * 8);
    if (h == 7) {
      u64 ep;
      const gu64* epp = ws + G_EPOCH;
      asm volatile("global_load_dwordx2 %0, %1, off sc1" : "=v"(ep) : "v"(epp) : "memory");
      wg_barrier();
      asm volatile("s_waitcnt vmcnt(0)" : "+v"(ep) :: "memory");
      const u64 ne = ep * 6364136223846793005ull + 1442695040888963407ull;
      if (l == 0) *(LAS u32x4*)(L + L_TAG) = u32x4{(u32)(ne >> 8), (u32)ne & 0xffu, (u32)ne, (u32)(ne >> 32)};
      cnt_add(cnt0 + 4 * C_WQ);
    } else {
      wg_barrier();
      if (h >= 5) {
        float xv[8];
#pragma unroll
        for (int k = 0; k < 4; k++) { xv[2 * k] = bflo(xr[k]); xv[2 * k + 1] = bfhi(xr[k]); }
        float sm = ((xv[0] + xv[1]) + (xv[2] + xv[3])) + ((xv[4] + xv[5]) + (xv[6] + xv[7]));
        const float mean = wave_sum(l < 56 ? sm : 0.f) * (1.f / 448.f);
        float ss = 0.f;
#pragma unroll
        for (int k = 0; k < 8; k++) { const float d = xv[k] - mean; ss = fmaf(d, d, ss); }
        const float var = rbf(wave_sum(l < 56 ? ss : 0.f) * (1.f / 448.f)), mb = rbf(mean);
        const float rden = __builtin_amdgcn_rcpf(rbf(__builtin_amdgcn_sqrtf(rbf(var + 1e-5f))));
        u32x4 o;
#pragma unroll
        for (int k = 0; k < 4; k++) o[k] = pk2bf(rbf(xv[2 * k] - mb) * rden, rbf(xv[2 * k + 1] - mb) * rden);
        const int rl = l - 14 * kq;
        if (rl >= 0 && rl < 14) *(LAS u32x4*)(L + L_XN + (h & 1) * 256 + rl * 16) = o;
        if (l >= 62) *(LAS u32x4*)(L + L_XN + (h & 1) * 256 + (l - 48) * 16) = u32x4{0, 0, 0, 0};
      }
      waitvm<0>();
      cnt_add(cnt0 + 4 * C_WQ);
    }
    cnt_spin(cnt + C_WQ, 8);
    tg.hi = __builtin_amdgcn_readfirstlane(*(LAS u32*)(L + L_TAG));
    tg.lo = __builtin_amdgcn_readfirstlane(*(LAS u32*)(L + L_TAG + 4));
    if (h < 4) {
      f32x4 acc = {0.f, 0.f, 0.f, 0.f};
#pragma unroll
      for (int ks = 0; ks < 4; ks++) {
        const u32x4 af = *(LAS u32x4*)(L + L_XN + (n & 1) * 256 + ks * 64 + g * 16);
        int r0 = 32 * ks + 8 * g + q4;
        if (ks == 3) r0 = (g >= 2) ? r0 - 16 : r0;
        const u32 a = lds0 + L_WQ + r0 * 128 + 32 * h + 8 * p4;
        acc = mfma16(af, tr_frag(a, a + 4 * 128), acc);
      }
      if (g < 2) put(ws + G_QP + (g * 4 + kq) * 4096 + 64 * cg + 16 * h + n, g ? acc[1] : acc[0], tg);
    }
    cnt_wait(cnt + C_KT, 8);
    float qg[4];
    sweep<4>(ws + G_QP + b * 4 * 4096 + kvh * 512 + 64 * h + l, 4096, tg, qg);
    *(LAS u16*)(L + L_Q + (64 * h + l) * 2) = f2bf((qg[0] + qg[1]) + (qg[2] + qg[3]));
    cnt_add(cnt0 + 4 * C_Q);
  }
  {
    cnt_wait(cnt + C_Q, 8);
    u32x4 qf[8];
#pragma unroll
    for (int j = 0; j < 8; j++) qf[j] = *(LAS u32x4*)(L + L_Q + (n & 1) * 512 + j * 64 + g * 16);
    cnt_wait(cnt + C_K, 8);
    const LAS unsigned char* kr = L + L_K + (32 * h + n) * 512;
    u32x4 kf[16];
#pragma unroll
    for (int j = 0; j < 8; j++) {
      kf[2 * j] = *(const LAS u32x4*)(kr + (((4 * j + g) ^ n) * 16));
      kf[2 * j + 1] = *(const LAS u32x4*)(kr + 8192 + (((4 * j + g) ^ n) * 16));
    }
    __builtin_amdgcn_sched_barrier(0);
    f32x4 acc0 = {0.f, 0.f, 0.f, 0.f}, acc1 = {0.f, 0.f, 0.f, 0.f};
#pragma unroll
    for (int j = 0; j < 8; j++) { acc0 = mfma16(qf[j], kf[2 * j], acc0); acc1 = mfma16(qf[j], kf[2 * j + 1], acc1); }
    const float sr = (g & 1) ? ((g >> 1) ? acc1[1] : acc1[0]) : ((g >> 1) ? acc0[1] : acc0[0]);
    const float sc = rbf(sr) * 0.0625f;
    const float mw = x16_max(row_max(sc));
    LAS float* sm = (LAS float*)(L + L_SM);
    if ((l & 31) == 0) sm[(g >> 1) * 8 + h] = mw;
    cnt_add(cnt0 + 4 * C_MAX);
    cnt_spin(cnt + C_MAX, 8);
    const f32x4 ma = *(LAS f32x4*)(sm + (g >> 1) * 8), mb = *(LAS f32x4*)(sm + (g >> 1) * 8 + 4);
    *(LAS u32x4*)(L + L_RED + h * 2048 + l * 32) = u32x4{~0u, ~0u, ~0u, ~0u};
    *(LAS u32x4*)(L + L_RED + h * 2048 + l * 32 + 16) = u32x4{~0u, ~0u, ~0u, ~0u};
    const float m = fmaxf(fmaxf(fmaxf(ma[0], ma[1]), fmaxf(ma[2], ma[3])), fmaxf(fmaxf(mb[0], mb[1]), fmaxf(mb[2], mb[3])));
    const u16 pb = f2bf(__builtin_amdgcn_exp2f((sc - m) * 1.4426950408889634f));
    const int row = 16 * (g & 1) + n;
    *(LAS u16*)(L + L_P + (g >> 1) * 512 + (row >> 2) * 64 + (row & 3) * 16 + ((h - s) & 7) * 2) = pb;
    const float lw = x16_sum(row_sum(bflo((u32)pb)));
    if ((l & 31) == 0) sm[16 + (g >> 1) * 8 + h] = lw;
    cnt_add(cnt0 + 4 * C_P);
    if (h == 7) {
      cnt_spin(cnt + C_P, 8);
      const f32x4 la = *(LAS f32x4*)(sm + 16 + (g >> 1) * 8), lb = *(LAS f32x4*)(sm + 16 + (g >> 1) * 8 + 4);
      const float ls = ((la[0] + la[1]) + (la[2] + la[3])) + ((lb[0] + lb[1]) + (lb[2] + lb[3]));
      if ((l & 30) == 0) put(ws + G_S1 + (((kvh * 2 + (g >> 1)) * 2 + b) * 16 + s) * 2 + (l & 1), (l & 1) ? ls : m, tg);
    }
  }
  const Tag tgo{tg.hi, tg.lo ^ 2u};
  const u32 tgy = tg.hi ^ 0x5bd1e995u;
  const f32x4 z4 = {0.f, 0.f, 0.f, 0.f};
  constexpr u32 SLO = 0x05040100u, SHI = 0x07060302u;
  if (idx < 16) {
    const int r1 = (idx >> 2) * 8 + xcd, cq = idx & 3;
    const bool mm = h < (cq == 3 ? 2 : 4);
    cnt_wait(cnt + C_VISS, 8);
    {
      const unsigned char* wsrc = (const unsigned char*)P.wo + (size_t)(r1 * 128 + 16 * h) * 896 + 256 * cq;
#pragma unroll
      for (int dd = 0; dd < 4; dd++) {
        const int ch = (n ^ ((g << 1) | (((dd >> 1) & 1) << 3))) & (cq == 3 ? 7 : 15);
        glds16(wsrc + dd * 4 * 896, g * 896 + ch * 16, lds0 + L_K + (4 * h + dd) * 1024);
      }
    }
    const gu64* pd = ws + G_P1 + (4 * r1 + (h >> 1)) * 512 + (h & 1) * 256 + 2 * l;
    constexpr int NP = 2;
    const u32 tgp = tg.hi ^ 0x2545f491u;
    const gu64* ps = ws + G_S1 + ((r1 >> 1) * 2 + (h & 1)) * 32 + (l & 31);
    u32x4 wf[4][2];
    waitvm<0>();
    cnt_add(cnt0 + 4 * C_W);
    if (mm) {
      cnt_wait(cnt + C_W, 8);
      const u32 a0 = lds0 + L_K + (8 * g + q4) * 256 + (((4 * h + (p4 >> 1)) ^ ((q4 << 1) | ((g & 1) << 3))) * 16) + 8 * (p4 & 1);
#pragma unroll
      for (int t = 0; t < 2; t++) {
        u32x2 r[8];
        asm volatile("ds_read_b64_tr_b16 %0, %8\n\tds_read_b64_tr_b16 %1, %8 offset:1024\n\tds_read_b64_tr_b16 %2, %8 offset:8192\n\tds_read_b64_tr_b16 %3, %8 offset:9216\n\t"
                     "ds_read_b64_tr_b16 %4, %8 offset:16384\n\tds_read_b64_tr_b16 %5, %8 offset:17408\n\tds_read_b64_tr_b16 %6, %8 offset:24576\n\tds_read_b64_tr_b16 %7, %8 offset:25600\n\t"
                     "s_waitcnt lgkmcnt(0)"
                     : "=&v"(r[0]), "=&v"(r[1]), "=&v"(r[2]), "=&v"(r[3]), "=&v"(r[4]), "=&v"(r[5]), "=&v"(r[6]), "=&v"(r[7]) : "v"(a0 ^ (32 * t)) : "memory");
#pragma unroll
        for (int ks = 0; ks < 4; ks++) wf[ks][t] = u32x4{r[2 * ks].x, r[2 * ks].y, r[2 * ks + 1].x, r[2 * ks + 1].y};
      }
    }
    gu64* dst = ws + G_QP + (2 * (r1 & 3) + (g & 1)) * 4096 + (r1 >> 2) * 512 + 64 * cq + 16 * h + 8 * (g >> 1) + (n >> 1);
    const LAS u32x4* ap = (const LAS u32x4*)(L + L_OB + (n & 1) * 256 + g * 16);
    asm volatile("" : "+v"(dst), "+v"(ap));
    cnt_wait(cnt + C_DONE, 8);
    u32x4 pv[2];
    u64 sg;
    float wk[2];
    u32 pend = 0x10u | ((1u << NP) - 1u);
    for (int spins = 0; spins < 4 * SPIN_CAP && pend; spins++) {
#pragma unroll
      for (int k = 0; k < NP; k++) if (pend & (1u << k))
        asm volatile("global_load_dwordx4 %0, %1, off offset:%2 sc1" : "=v"(pv[k]) : "v"(pd), "i"(k * 1024) : "memory");
      if (pend & 0x10u) asm volatile("global_load_dwordx2 %0, %1, off sc1" : "=v"(sg) : "v"(ps) : "memory");
      asm volatile("s_waitcnt vmcnt(0)" : "+v"(pv[0]), "+v"(pv[1]), "+v"(sg) :: "memory");
#pragma unroll
      for (int k = 0; k < NP; k++) if (pend & (1u << k)) {
        if (__all(((pv[k][1] ^ tgp) | (pv[k][3] ^ tgp)) == 0u)) pend &= ~(1u << k);
      }
      if ((pend & 0x10u) && __all(tag_bad(sg, tg) == 0u)) {
        pend &= 0xfu;
        const float st = dec(sg);
        const float M = x16_max(row_max((l & 1) ? -INFINITY : st));
        const float wgt = __builtin_amdgcn_exp2f((st - M) * 1.4426950408889634f);
        const float lw = dpp<0xB1>(wgt) * st;
        const float wn = wgt * __builtin_amdgcn_rcpf(x16_sum(row_sum((l & 1) ? lw : 0.f)));
#pragma unroll
        for (int k = 0; k < NP; k++) wk[k] = asf(__builtin_amdgcn_ds_bpermute(8 * (8 * k + (l >> 3)), asu(wn)));
      }
    }
    {
      float o[4];
      o[0] = fmaf(wk[1], bflo(pv[1][0]), wk[0] * bflo(pv[0][0])); o[1] = fmaf(wk[1], bfhi(pv[1][0]), wk[0] * bfhi(pv[0][0]));
      o[2] = fmaf(wk[1], bflo(pv[1][2]), wk[0] * bflo(pv[0][2])); o[3] = fmaf(wk[1], bfhi(pv[1][2]), wk[0] * bfhi(pv[0][2]));
#pragma unroll
      for (int e = 0; e < 4; e++) { o[e] += dpp<0x128>(o[e]); o[e] = x32_sum(x16_sum(o[e])); }
      if (l < 8) *(LAS u32x2*)(L + L_OB + (h & 1) * 256 + (h >> 1) * 64 + l * 8) = u32x2{pk2bf(o[0], o[1]), pk2bf(o[2], o[3])};
    }
    wg_barrier();
    if (mm) {
      u32x4 af[4];
#pragma unroll
      for (int ks = 0; ks < 4; ks++) af[ks] = ap[4 * ks];
      f32x4 d0 = z4, d1 = z4;
#pragma unroll
      for (int ks = 0; ks < 4; ks++) { d0 = mfma16(af[ks], wf[ks][0], d0); d1 = mfma16(af[ks], wf[ks][1], d1); }
      const float yv1 = (g >> 1) ? ((g & 1) ? d1[1] : d1[0]) : ((g & 1) ? d0[1] : d0[0]);
      const u32 pk = pk2bf(yv1, dpp<0xB1>(yv1));
      if (!(l & 1)) __hip_atomic_store(dst, ((u64)tgy << 32) | pk, RLX_AGENT);
    }
    if (h == 7 || cq < 2 || r1 >= 28) return;
    const int t = 2 * r1 + cq - 2, tb = t >= 28 ? 1 : 0, col = 16 * t - 448 * tb;
    u32 hpk = 0;
    for (int spins = 0; spins < SPIN_CAP; spins++) {
      const u64 gg = __hip_atomic_load(ws + G_H + 8 * t + (n >> 1), RLX_AGENT);
      hpk = (u32)gg;
      if (__all((u32)(gg >> 32) == tgy)) break;
      __builtin_amdgcn_s_sleep(4);
    }
    float fv[2];
    sweep<2>(ws + G_P1 + (tb * 56 + 8 * h + g) * OPB + col + n, 4 * OPB, tgo, fv);
    const float y = x32_sum(x16_sum(fv[0] + fv[1]));
    if (l < 16) *(LAS float*)(L + L_UR + h * 64 + n * 4) = y;
    wg_barrier();
    if (h == 0) {
      float yy = 0.f;
#pragma unroll
      for (int k = 0; k < 7; k++) yy += *(LAS float*)(L + L_UR + k * 64 + n * 4);
      if (l < 16) P.out[16 * t + n] = f2bf(((n & 1) ? bfhi(hpk) : bflo(hpk)) + rbf(yy));
      if (t == 0 && l == 0) __hip_atomic_store(ws + G_EPOCH, *(LAS u64*)(L + L_TAG + 8), RLX_AGENT);
    }
  } else if (xcd < 7) {
    if (h == 7) return;
    const int fs = xcd * 8 + ((idx - 16) >> 1), fb = idx & 1;
    cnt_wait(cnt + C_DONE, 8);
    {
      const unsigned char* w1s = (const unsigned char*)P.w1 + fs * 64;
      const unsigned char* w2s = (const unsigned char*)P.w2 + (size_t)fs * 32 * 896;
#pragma unroll
      for (int d = 0; d < 4; d++) glds16(w1s + (size_t)(h * 4 + d) * 16 * 3584, (l >> 2) * 3584 + (l & 3) * 16, lds0 + L_K + (h * 4 + d) * 1024);
#pragma unroll
      for (int d = 0; d < 4; d++) glds16(w2s + (h * 4 + d) * 1024, l * 16, lds0 + L_K + 28672 + (h * 4 + d) * 1024);
    }
    const u32x2 xw = *(const u32x2*)(P.x + fb * 448 + 64 * h + 4 * n);
    waitvm<0>();
    cnt_add(cnt0 + 4 * C_W);
    u32x4 w1f[2][2], w2f[4];
    {
      const u32 a1 = lds0 + L_K + (64 * h + 8 * g + q4) * 64 + 8 * p4;
      u32x2 r[8];
      asm volatile("ds_read_b64_tr_b16 %0, %8\n\tds_read_b64_tr_b16 %1, %8 offset:256\n\tds_read_b64_tr_b16 %2, %8 offset:32\n\tds_read_b64_tr_b16 %3, %8 offset:288\n\t"
                   "ds_read_b64_tr_b16 %4, %8 offset:2048\n\tds_read_b64_tr_b16 %5, %8 offset:2304\n\tds_read_b64_tr_b16 %6, %8 offset:2080\n\tds_read_b64_tr_b16 %7, %8 offset:2336\n\t"
                   "s_waitcnt lgkmcnt(0)"
                   : "=&v"(r[0]), "=&v"(r[1]), "=&v"(r[2]), "=&v"(r[3]), "=&v"(r[4]), "=&v"(r[5]), "=&v"(r[6]), "=&v"(r[7]) : "v"(a1) : "memory");
#pragma unroll
      for (int k = 0; k < 4; k++) w1f[k >> 1][k & 1] = u32x4{r[2 * k].x, r[2 * k].y, r[2 * k + 1].x, r[2 * k + 1].y};
      cnt_spin(cnt + C_W, 7);
      const u32 a2 = lds0 + L_K + 28672 + (8 * g + q4) * 896 + 128 * h + 8 * p4;
      asm volatile("ds_read_b64_tr_b16 %0, %8\n\tds_read_b64_tr_b16 %1, %8 offset:3584\n\tds_read_b64_tr_b16 %2, %8 offset:32\n\tds_read_b64_tr_b16 %3, %8 offset:3616\n\t"
                   "ds_read_b64_tr_b16 %4, %8 offset:64\n\tds_read_b64_tr_b16 %5, %8 offset:3648\n\tds_read_b64_tr_b16 %6, %8 offset:96\n\tds_read_b64_tr_b16 %7, %8 offset:3680\n\t"
                   "s_waitcnt lgkmcnt(0)"
                   : "=&v"(r[0]), "=&v"(r[1]), "=&v"(r[2]), "=&v"(r[3]), "=&v"(r[4]), "=&v"(r[5]), "=&v"(r[6]), "=&v"(r[7]) : "v"(a2) : "memory");
#pragma unroll
      for (int k = 0; k < 4; k++) w2f[k] = u32x4{r[2 * k].x, r[2 * k].y, r[2 * k + 1].x, r[2 * k + 1].y};
    }
    const u32x4 ones = {0x3f803f80u, 0x3f803f80u, 0x3f803f80u, 0x3f803f80u};
    gu64* dst = ws + G_P1 + (fb * 56 + fs) * OPB + 64 * h + l;
    const gu64* src = ws + G_QP + (2 * g + fb) * 4096 + 32 * h + 2 * n;
    constexpr int YS = 512;
    asm volatile("" : "+v"(dst), "+v"(src));
#pragma unroll
    for (int e = 0; e < 4; e++) asm volatile("" : "+v"(w2f[e]));
    asm volatile("" : "+v"(w1f[0][0]), "+v"(w1f[0][1]), "+v"(w1f[1][0]), "+v"(w1f[1][1]));
    u32x4 yv[8];
    {
      u32 pend = 0xffu;
      for (int spins = 0; spins < 4 * SPIN_CAP && pend; spins++) {
#pragma unroll
        for (int k = 0; k < 8; k++) if (pend & (1u << k))
          asm volatile("global_load_dwordx4 %0, %1, off sc1" : "=v"(yv[k]) : "v"(src + k * YS) : "memory");
        asm volatile("s_waitcnt vmcnt(0)" : "+v"(yv[0]), "+v"(yv[1]), "+v"(yv[2]), "+v"(yv[3]), "+v"(yv[4]), "+v"(yv[5]), "+v"(yv[6]), "+v"(yv[7]) :: "memory");
#pragma unroll
        for (int k = 0; k < 8; k++) if (pend & (1u << k)) {
          if (__all(((yv[k][1] ^ tgy) | (yv[k][3] ^ tgy)) == 0u)) pend &= ~(1u << k);
        }
      }
    }
    u32x2 hp;
    {
      float y[4];
#pragma unroll
      for (int e = 0; e < 4; e++) {
        u32x4 bq;
#pragma unroll
        for (int t = 0; t < 4; t++) bq[t] = __builtin_amdgcn_perm(yv[2 * t + 1][e & 2], yv[2 * t][e & 2], (e & 1) ? SHI : SLO);
        y[e] = mfma16(ones, bq, z4)[0];
      }
      hp.x = pk2bf(bflo(xw.x) + rbf(y[0]), bfhi(xw.x) + rbf(y[1]));
      hp.y = pk2bf(bflo(xw.y) + rbf(y[2]), bfhi(xw.y) + rbf(y[3]));
      if (l < 16) *(LAS u32x2*)(L + L_FB + h * 128 + n * 8) = hp;
    }
    waitlgkm();
    const u32x4 af0 = *(LAS u32x4*)(L + L_FB + h * 128 + g * 16), af1 = *(LAS u32x4*)(L + L_FB + h * 128 + 64 + g * 16);
    if (fs == 0 && l < 32) __hip_atomic_store(ws + G_H + fb * 224 + 32 * h + 2 * n + g, ((u64)tgy << 32) | (g ? hp.y : hp.x), RLX_AGENT);
    f32x4 u0, u1, st;
    {
      const u32x4 a0 = (n & 1) ? ones : af0, a1 = (n & 1) ? ones : af1;
      u0 = mfma16(a0, w1f[0][0], z4); u1 = mfma16(a0, w1f[0][1], z4); st = mfma16(a0, af0, z4);
      u0 = mfma16(a1, w1f[1][0], u0); u1 = mfma16(a1, w1f[1][1], u1); st = mfma16(a1, af1, st);
    }
    {
      const f32x2 uv = (g == 0) ? f32x2{u0[0], u0[1]} : (g == 1) ? f32x2{u1[0], u1[1]} : f32x2{st[0], st[1]};
      if (l < 33) *(LAS f32x2*)(L + L_UR + h * 320 + l * 8) = uv;
    }
    wg_barrier();
    {
      f32x2 uc = {0.f, 0.f}, sa = {0.f, 0.f};
#pragma unroll
      for (int k = 0; k < 7; k++) {
        uc += *(LAS f32x2*)(L + L_UR + k * 320 + (l & 31) * 8);
        sa += *(LAS f32x2*)(L + L_UR + k * 320 + 256);
      }
      const float ua = uc[0], cs = uc[1];
      const float mean = sa[1] * (1.f / 448.f);
      const float var = rbf(fmaxf(fmaf(-mean, mean, sa[0] * (1.f / 448.f)), 0.f));
      const float rden = __builtin_amdgcn_rcpf(rbf(__builtin_amdgcn_sqrtf(rbf(var + 1e-5f))));
      const float u = rbf(fmaf(-rbf(mean), cs, ua) * rden);
      const float f = u * __builtin_amdgcn_rcpf(1.f + __builtin_amdgcn_exp2f(-u * 1.4426950408889634f));
      if (l < 32) *(LAS u16*)(L + L_FB + h * 128 + l * 2) = f2bf(f);
    }
    waitlgkm();
    const u32x4 ff = *(LAS u32x4*)(L + L_FB + h * 128 + g * 16);
    f32x4 d[4];
#pragma unroll
    for (int e = 0; e < 4; e++) d[e] = mfma16(ff, w2f[e], z4);
    put(dst, (g >> 1) ? ((g & 1) ? d[3][0] : d[2][0]) : ((g & 1) ? d[1][0] : d[0][0]), tgo);
  }
}

extern "C" void kernel_launch(void* const* d_in, const int* in_sizes, int n_in, void* d_out, int out_size, void* d_ws, size_t ws_size,
                              hipStream_t stream) {
  Params p;
  p.x = (const u16*)d_in[0]; p.kc = (const u16*)d_in[1]; p.vc = (const u16*)d_in[2]; p.wq = (const u16*)d_in[3]; p.wo = (const u16*)d_in[4];
  p.w1 = (const u16*)d_in[6]; p.w2 = (const u16*)d_in[7]; p.out = (u16*)d_out; p.ws = (u64*)d_ws;
  fused_block_kernel<<<256, NT, 0, stream>>>(p);
}
```
